```python
import torch

def _softmax(x, axis=-1):
    return torch.softmax(x, dim=axis)
def _gelu(x):
    return torch.nn.functional.gelu(x, approximate="tanh")
def _silu(x):
    return torch.nn.functional.silu(x)
def _sigmoid(x):
    return torch.sigmoid(x)
def _layernorm(x, axis=-1, eps=1e-5):
    m = x.mean(dim=axis, keepdim=True)
    v = x.var(dim=axis, keepdim=True, unbiased=False)
    return (x - m) / torch.sqrt(v + eps)
def _scatter_add(tgt, idx, src, axis=0):
    return tgt.clone().index_add_(axis, idx, src)
def _segment_sum(src, seg, n):
    out = torch.zeros((n,) + src.shape[1:], dtype=src.dtype)
    return out.index_add_(0, seg, src)
def _pad(x, pw):
    flat = tuple(v for pair in reversed(pw) for v in pair)
    return torch.nn.functional.pad(x, flat)

kernel_name = 'concat_heads__gqa_block_cached__mha_block_cached__near_ridge'

def setup_inputs():

    _w = (lambda a: (lambda t: t.to(torch.bfloat16) if t.is_floating_point() else t)(torch.as_tensor(a)))
    def _A_setup():
        B, Sq, D = 8, 128, 640
        Hq, Hkv, Sk, Dh = 32, 32, 512, 128
        assert Hq % Hkv == 0, (Hq, Hkv)
        return {
            "x": _w((lambda *s: torch.randn(*s, dtype=torch.bfloat16) * 0.8)(B, Sq, D)),
            "k_cache": _w((lambda *s: torch.randn(*s, dtype=torch.bfloat16) * 0.8)(B, Hkv, Sk, Dh)),
            "v_cache": _w((lambda *s: torch.randn(*s, dtype=torch.bfloat16) * 0.05)(B, Hkv, Sk, Dh)),
            "wq": _w((lambda *s: torch.randn(*s, dtype=torch.bfloat16) * 0.8)(D, Hq * Dh) * (D ** -0.5)),
            "wo": _w((lambda *s: torch.randn(*s, dtype=torch.bfloat16) * 0.05)(Hq * Dh, D) * ((Hq * Dh) ** -0.5)),
        }
    _A_in = _A_setup()

    def _B_setup():
        B, Sq, D = 8, 128, 640
        H, Sk, Dh = 32, 512, 128
        return {
            "x": _w((lambda *s: torch.randn(*s, dtype=torch.bfloat16) * 0.8)(B, Sq, D)),
            "k_cache": _w((lambda *s: torch.randn(*s, dtype=torch.bfloat16) * 0.8)(B, H, Sk, Dh)),
            "v_cache": _w((lambda *s: torch.randn(*s, dtype=torch.bfloat16) * 0.05)(B, H, Sk, Dh)),
            "wq": _w((lambda *s: torch.randn(*s, dtype=torch.bfloat16) * 0.8)(D, H * Dh) * (D ** -0.5)),
            "wo": _w((lambda *s: torch.randn(*s, dtype=torch.bfloat16) * 0.05)(H * Dh, D) * ((H * Dh) ** -0.5)),
        }
    _B_in = _B_setup()

    return {
        "A_x": _A_in["x"],
        "A_k_cache": _A_in["k_cache"],
        "A_v_cache": _A_in["v_cache"],
        "A_wq": _A_in["wq"],
        "A_wo": _A_in["wo"],
        "B_x": _B_in["x"],
        "B_k_cache": _B_in["k_cache"],
        "B_v_cache": _B_in["v_cache"],
        "B_wq": _B_in["wq"],
        "B_wo": _B_in["wo"],
    }

def reference(A_x, A_k_cache, A_v_cache, A_wq, A_wo, B_x, B_k_cache, B_v_cache, B_wq, B_wo):
    def _A_ref(x, k_cache, v_cache, wq, wo):
        B, Sq, D = x.shape
        Hq, Dh = 32, 128
        Hkv = k_cache.shape[1]
        rep = Hq // Hkv
        q = (lambda x, *d: x.permute(*d))((x @ wq).reshape(B, Sq, Hq, Dh), 0, 2, 1, 3)
        k = (lambda x, n, axis: torch.repeat_interleave(x, n, dim=axis))(k_cache, rep, axis=1)
        v = (lambda x, n, axis: torch.repeat_interleave(x, n, dim=axis))(v_cache, rep, axis=1)
        scores = torch.einsum("bhqd,bhkd->bhqk", q, k) * (Dh ** -0.5)
        p = _softmax(scores, axis=-1)
        o = torch.einsum("bhqk,bhkd->bhqd", p, v)
        return (lambda x, *d: x.permute(*d))(o, 0, 2, 1, 3).reshape(B, Sq, Hq * Dh) @ wo

    def _B_ref(x, k_cache, v_cache, wq, wo):
        B, Sq, D = x.shape
        H, Dh = 32, 128
        q = (lambda x, *d: x.permute(*d))((x @ wq).reshape(B, Sq, H, Dh), 0, 2, 1, 3)
        scores = torch.einsum("bhqd,bhkd->bhqk", q, k_cache) * (Dh ** -0.5)
        p = _softmax(scores, axis=-1)
        o = torch.einsum("bhqk,bhkd->bhqd", p, v_cache)
        return (lambda x, *d: x.permute(*d))(o, 0, 2, 1, 3).reshape(B, Sq, H * Dh) @ wo

    a = _A_ref(A_x, A_k_cache, A_v_cache, A_wq, A_wo)
    b = _B_ref(B_x, B_k_cache, B_v_cache, B_wq, B_wo)
    return torch.concatenate([a, b], axis=-1)
```

```cpp
#include <hip/hip_runtime.h>
#include <hip/hip_bf16.h>

typedef __bf16 bf16x8 __attribute__((ext_vector_type(8)));
typedef __bf16 bf16x4 __attribute__((ext_vector_type(4)));
typedef __bf16 bf16x2 __attribute__((ext_vector_type(2)));
typedef float f32x16 __attribute__((ext_vector_type(16)));
typedef float f32x4 __attribute__((ext_vector_type(4)));
typedef float f32x2 __attribute__((ext_vector_type(2)));
typedef unsigned u32x4 __attribute__((ext_vector_type(4)));
typedef unsigned u32x2 __attribute__((ext_vector_type(2)));
#define LAS __attribute__((address_space(3)))
#define DI __device__ __forceinline__
#define MFMA32(a, b, c) __builtin_amdgcn_mfma_f32_32x32x16_bf16((a), (b), (c), 0, 0, 0)
#define MFMA16(a, b, c) __builtin_amdgcn_mfma_f32_16x16x32_bf16((a), (b), (c), 0, 0, 0)

constexpr int NB = 8, SQ = 128, DM = 640, NH = 32, SK = 512, DH = 128;
constexpr int SLOT = 16384, NSLOT = 5;
constexpr float QSCALE = 0.088388347648318440f * 1.4426950408889634f;
constexpr float THR = 10.0f;

struct Params {
  const char* x[2]; const char* k[2]; const char* v[2]; const char* wq[2]; const char* wo[2];
  char* ows; char* out;
};

DI unsigned cvtpk(float lo, float hi) { f32x2 v = {lo, hi}; bf16x2 b = __builtin_convertvector(v, bf16x2); return __builtin_bit_cast(unsigned, b); }
DI bf16x8 pack8(const f32x16& a, int s, float sc) {
  u32x4 w = {cvtpk(a[8 * s] * sc, a[8 * s + 1] * sc), cvtpk(a[8 * s + 2] * sc, a[8 * s + 3] * sc),
             cvtpk(a[8 * s + 4] * sc, a[8 * s + 5] * sc), cvtpk(a[8 * s + 6] * sc, a[8 * s + 7] * sc)};
  return __builtin_bit_cast(bf16x8, w);
}
DI bf16x8 pack8(const f32x16& a, int s) {
  u32x4 w = {cvtpk(a[8 * s], a[8 * s + 1]), cvtpk(a[8 * s + 2], a[8 * s + 3]), cvtpk(a[8 * s + 4], a[8 * s + 5]), cvtpk(a[8 * s + 6], a[8 * s + 7])};
  return __builtin_bit_cast(bf16x8, w);
}
DI bf16x8 tr_frag(unsigned addr, int off0, int off1) {
  bf16x4 a = __builtin_amdgcn_ds_read_tr16_b64_v4bf16((LAS bf16x4*)(addr + off0));
  bf16x4 b = __builtin_amdgcn_ds_read_tr16_b64_v4bf16((LAS bf16x4*)(addr + off1));
  return __builtin_shufflevector(a, b, 0, 1, 2, 3, 4, 5, 6, 7);
}
DI bf16x8 lds_b128(unsigned addr) { return *(LAS bf16x8*)addr; }
typedef __amdgpu_buffer_rsrc_t rsrc_t;
DI rsrc_t mk_rsrc(const void* p, unsigned bytes) { return __builtin_amdgcn_make_buffer_rsrc((void*)p, (short)0, (int)bytes, 0x00020000); }

DI void dma1(unsigned m0v, unsigned voff, rsrc_t rs, unsigned soff) {
  asm volatile("s_mov_b32 m0, %0\n\ts_nop 0\n\tbuffer_load_dwordx4 %1, %2, %3 offen lds" ::"s"(m0v), "v"(voff), "s"(rs), "s"(soff) : "memory");
}
DI void dma4(unsigned m0v, unsigned v0, unsigned v1, unsigned v2, unsigned v3, rsrc_t rs, unsigned soff) {
  asm volatile("s_mov_b32 m0, %0\n\ts_nop 0\n\t"
               "buffer_load_dwordx4 %1, %5, %6 offen lds\n\t"
               "buffer_load_dwordx4 %2, %5, %6 offen offset:1024 lds\n\t"
               "buffer_load_dwordx4 %3, %5, %6 offen offset:2048 lds\n\t"
               "buffer_load_dwordx4 %4, %5, %6 offen offset:3072 lds"
               ::"s"(m0v), "v"(v0), "v"(v1), "v"(v2), "v"(v3), "s"(rs), "s"(soff) : "memory");
}
template <int OFF> DI void xload(bf16x8& d, unsigned voff, rsrc_t rs) {
  asm volatile("buffer_load_dwordx4 %0, %1, %2, 0 offen offset:%3" : "=v"(d) : "v"(voff), "s"(rs), "i"(OFF) : "memory");
}
template <int N> DI void wait_bar() { asm volatile("s_waitcnt vmcnt(%0) lgkmcnt(0)\n\ts_barrier" ::"i"(N) : "memory"); }
template <int N> DI void wait_bar_x(bf16x8 (&x)[4]) {
  asm volatile("s_waitcnt vmcnt(%4) lgkmcnt(0)\n\ts_barrier" : "+v"(x[0]), "+v"(x[1]), "+v"(x[2]), "+v"(x[3]) : "i"(N) : "memory");
}

struct Ctx {
  rsrc_t rk, rv, rw, rx;
  unsigned lds, m0w;
  unsigned voffW, voffV, voffK[4], voffX;
  unsigned wbase, vbase, kbase;
};

template <int I> DI void issue_w(Ctx& c, bf16x8 (&xf)[4]) {
  xload<I * 128 + 0>(xf[0], c.voffX, c.rx); xload<I * 128 + 32>(xf[1], c.voffX, c.rx);
  xload<I * 128 + 64>(xf[2], c.voffX, c.rx); xload<I * 128 + 96>(xf[3], c.voffX, c.rx);
#pragma unroll
  for (int i = 0; i < 4; ++i) dma1(c.m0w + (I % NSLOT) * SLOT + i * 1024, c.voffW, c.rw, (unsigned)(64 * I + 4 * i) * 8192u);
}
DI void issue_k(Ctx& c, int j, unsigned slot) { dma4(c.m0w + slot, c.voffK[0], c.voffK[1], c.voffK[2], c.voffK[3], c.rk, (unsigned)j * 16384u); }
DI void issue_v(Ctx& c, int j, unsigned slot) { dma4(c.m0w + slot, c.voffV, c.voffV, c.voffV, c.voffV, c.rv, (unsigned)j * 16384u); }

constexpr int item_ops(int i) { return i > 25 ? 0 : (i < 10 ? 8 : 4); }
constexpr int wait_n(int c) { return c == 0 ? 32 : item_ops(c + 1) + item_ops(c + 2) + item_ops(c + 3); }

template <int C> DI void qstep(Ctx& c, bf16x8 (&xf)[5][4], f32x16 (&qa)[4]) {
  wait_bar_x<wait_n(C)>(xf[C % 5]);
  if constexpr (C >= 1) {
    constexpr int I = C + 4;
    if constexpr (I < 10) issue_w<I>(c, xf[I % 5]);
    else if constexpr ((I & 1) == 0) issue_k(c, (I - 10) >> 1, (I % NSLOT) * SLOT);
    else issue_v(c, (I - 10) >> 1, (I % NSLOT) * SLOT);
  }
  const unsigned base = c.wbase + (C % NSLOT) * SLOT;
#pragma unroll
  for (int s = 0; s < 4; ++s)
#pragma unroll
    for (int dt = 0; dt < 4; ++dt) {
      bf16x8 a = tr_frag(base, s * 4096 + dt * 256, s * 4096 + 1024 + dt * 256);
      qa[dt] = MFMA32(a, xf[C % 5][s], qa[dt]);
    }
}

__global__ __launch_bounds__(256, 2) void attn_kernel(Params p) {
  extern __shared__ __attribute__((aligned(16))) char smem[];
  const int tid = threadIdx.x, lane = tid & 63;
  const int w = __builtin_amdgcn_readfirstlane(tid >> 6);
  const int r = lane & 31, h = lane >> 5;
  const int bid = blockIdx.x, xcd = bid & 7, idx = bid >> 3;
  const int br = xcd >> 2, head = (xcd & 3) * 8 + (idx >> 3), b = idx & 7;
  Ctx c;
  c.rk = mk_rsrc(p.k[br] + (size_t)(b * NH + head) * (SK * DH * 2), SK * DH * 2);
  c.rv = mk_rsrc(p.v[br] + (size_t)(b * NH + head) * (SK * DH * 2), SK * DH * 2);
  c.rw = mk_rsrc(p.wq[br] + head * 256, DM * 8192 - head * 256);
  c.rx = mk_rsrc(p.x[br] + (size_t)b * (SQ * DM * 2), SQ * DM * 2);
  c.lds = (unsigned)(size_t)(LAS char*)smem;
  c.m0w = c.lds + w * 4096;
  const int q4 = (lane >> 2) & 3, p4 = lane & 3, g1 = (lane >> 4) & 1;
  c.voffW = (16 * w + q4) * 8192 + (lane >> 4) * 64 + p4 * 16;
  c.voffV = (16 * w + q4) * 256 + (lane >> 4) * 64 + p4 * 16;
#pragma unroll
  for (int i = 0; i < 4; ++i) c.voffK[i] = (16 * w + (lane >> 4)) * 256 + (((lane & 15) ^ (4 * i + (lane >> 4))) * 16);
  c.voffX = (32 * w + r) * (DM * 2) + h * 16;
  const int p4s = ((p4 & 1) << 1) | (p4 >> 1);
  c.wbase = c.lds + h * 2048 + q4 * 64 + g1 * 32 + p4s * 8;
  c.vbase = c.lds + h * 1024 + q4 * 64 + g1 * 32 + p4 * 8;
  c.kbase = c.lds + r * 256 + ((h ^ (r & 15)) * 16);

  bf16x8 xf[5][4];
  issue_w<0>(c, xf[0]); issue_w<1>(c, xf[1]); issue_w<2>(c, xf[2]); issue_w<3>(c, xf[3]); issue_w<4>(c, xf[4]);
  f32x16 qa[4] = {};
  qstep<0>(c, xf, qa); qstep<1>(c, xf, qa); qstep<2>(c, xf, qa); qstep<3>(c, xf, qa); qstep<4>(c, xf, qa);
  qstep<5>(c, xf, qa); qstep<6>(c, xf, qa); qstep<7>(c, xf, qa); qstep<8>(c, xf, qa); qstep<9>(c, xf, qa);
  bf16x8 qf[8];
#pragma unroll
  for (int s8 = 0; s8 < 8; ++s8) qf[s8] = pack8(qa[s8 >> 1], s8 & 1, QSCALE);

  f32x16 o[4] = {};
  float m = -1e30f, l = 0.f;
  unsigned slotK = 0 * SLOT;
  for (int j = 0; j < 8; ++j) {
    const unsigned slotV = slotK == 4 * SLOT ? 0 : slotK + SLOT;
    const unsigned slotP = slotK == 0 ? 4 * SLOT : slotK - SLOT;
    if (j < 7) wait_bar<12>(); else wait_bar<4>();
    if (j < 6) issue_k(c, j + 2, slotP);
    f32x16 s0 = {}, s1 = {};
    {
      const unsigned kb = c.kbase + slotK;
#pragma unroll
      for (int s8 = 0; s8 < 8; ++s8) {
        const unsigned a = kb ^ (32 * s8);
        bf16x8 k0 = lds_b128(a), k1 = lds_b128(a + 8192);
        s0 = MFMA32(k0, qf[s8], s0);
        s1 = MFMA32(k1, qf[s8], s1);
      }
    }
    float pm = s0[0];
#pragma unroll
    for (int i = 1; i < 16; ++i) pm = fmaxf(pm, s0[i]);
#pragma unroll
    for (int i = 0; i < 16; ++i) pm = fmaxf(pm, s1[i]);
    { auto rr = __builtin_amdgcn_permlane32_swap(__float_as_uint(pm), __float_as_uint(pm), false, false);
      pm = fmaxf(__uint_as_float(rr[0]), __uint_as_float(rr[1])); }
    if (!__all(pm - m <= THR)) {
      const float mn = fmaxf(m, pm), al = __builtin_amdgcn_exp2f(m - mn);
      m = mn; l *= al;
#pragma unroll
      for (int dt = 0; dt < 4; ++dt)
#pragma unroll
        for (int i = 0; i < 16; ++i) o[dt][i] *= al;
    }
    float ps = 0.f;
#pragma unroll
    for (int i = 0; i < 16; ++i) { s0[i] = __builtin_amdgcn_exp2f(s0[i] - m); ps += s0[i]; }
#pragma unroll
    for (int i = 0; i < 16; ++i) { s1[i] = __builtin_amdgcn_exp2f(s1[i] - m); ps += s1[i]; }
    l += ps;
    bf16x8 pf[4] = {pack8(s0, 0), pack8(s0, 1), pack8(s1, 0), pack8(s1, 1)};
    if (j < 6) wait_bar<12>(); else if (j == 6) wait_bar<8>(); else wait_bar<0>();
    if (j < 6) issue_v(c, j + 2, slotK);
    {
      const unsigned vb = c.vbase + slotV;
#pragma unroll
      for (int ks = 0; ks < 4; ++ks)
#pragma unroll
        for (int dt = 0; dt < 4; ++dt) {
          bf16x8 a = tr_frag(vb, ks * 4096 + dt * 256, ks * 4096 + 2048 + dt * 256);
          o[dt] = MFMA32(a, pf[ks], o[dt]);
        }
    }
    slotK = slotV == 4 * SLOT ? 0 : slotV + SLOT;
  }
  { auto rr = __builtin_amdgcn_permlane32_swap(__float_as_uint(l), __float_as_uint(l), false, false);
    l = __uint_as_float(rr[0]) + __uint_as_float(rr[1]); }
  const float il = 1.0f / l;
  char* ob = p.ows + ((size_t)(((br * NB + b) * 4 + w) * NH + head) * 8) * 1024 + lane * 16;
#pragma unroll
  for (int f = 0; f < 8; ++f) *(bf16x8*)(ob + f * 1024) = pack8(o[f >> 1], f & 1, il);
}

constexpr int K2_STAGES = 4, K2_STAGE_B = 5120, K2_WAVE_B = K2_STAGES * K2_STAGE_B;
template <int OFF> DI void oload(bf16x8& d, unsigned voff, rsrc_t rs, unsigned soff) {
  asm volatile("buffer_load_dwordx4 %0, %1, %2, %3 offen offset:%4" : "=v"(d) : "v"(voff), "s"(rs), "s"(soff), "i"(OFF) : "memory");
}
struct Ctx2 { rsrc_t ro, rw; unsigned m0w, voffO, voffW[5], tbase; };
template <int ST> DI void k2_issue(Ctx2& c, int ks, bf16x8 (&of)[4]) {
  const unsigned so = (unsigned)ks * 2048u;
  oload<0>(of[0], c.voffO, c.ro, so); oload<256>(of[1], c.voffO, c.ro, so);
  oload<0>(of[2], c.voffO, c.ro, so + NH * 8192u); oload<256>(of[3], c.voffO, c.ro, so + NH * 8192u);
  const unsigned sw = (unsigned)ks * (32u * DM * 2);
#pragma unroll
  for (int i = 0; i < 5; ++i) dma1(c.m0w + ST * K2_STAGE_B + i * 1024, c.voffW[i], c.rw, sw);
}
template <int N> DI void k2_wait(bf16x8 (&x)[4]) {
  asm volatile("s_waitcnt vmcnt(%4)" : "+v"(x[0]), "+v"(x[1]), "+v"(x[2]), "+v"(x[3]) : "i"(N) : "memory");
}
template <int ST> DI void k2_compute(Ctx2& c, bf16x8 (&of)[4], f32x4 (&acc)[5][4]) {
#pragma unroll
  for (int nt = 0; nt < 5; ++nt) {
    bf16x8 a = tr_frag(c.tbase + ST * K2_STAGE_B, nt * 32, nt * 32 + 8 * 160);
#pragma unroll
    for (int t = 0; t < 4; ++t) acc[nt][t] = MFMA16(a, of[t], acc[nt][t]);
  }
  asm volatile("s_waitcnt lgkmcnt(0)" ::: "memory");
}

__global__ __launch_bounds__(512, 2) void oproj_kernel(Params p) {
  extern __shared__ __attribute__((aligned(16))) char smem[];
  const int tid = threadIdx.x, lane = tid & 63;
  const int w = __builtin_amdgcn_readfirstlane(tid >> 6);
  const int bid = blockIdx.x, xcd = bid & 7, idx = bid >> 3;
  const int br = xcd >> 2, mt = (xcd & 1) * 8 + (idx & 7), nt0 = ((xcd >> 1) & 1) * 4 + (idx >> 3);
  const int r16 = lane & 15, g = lane >> 4;
  Ctx2 c;
  c.ro = mk_rsrc(p.ows + ((size_t)(br * 32 + mt * 2) * NH + 4 * w) * 8192, 2 * NH * 8192 - 4 * w * 8192);
  c.rw = mk_rsrc(p.wo[br] + (size_t)(512 * w) * (DM * 2) + nt0 * 160, 512 * DM * 2 - nt0 * 160);
  const unsigned lds = (unsigned)(size_t)(LAS char*)smem;
  c.m0w = lds + w * K2_WAVE_B;
  c.voffO = (g >> 1) * 1024 + ((g & 1) * 32 + r16) * 16;
#pragma unroll
  for (int i = 0; i < 5; ++i) { const int ch = i * 64 + lane, row = ch / 10, cc = ch % 10; c.voffW[i] = row * (DM * 2) + cc * 16; }
  c.tbase = lds + w * K2_WAVE_B + (16 * (g >> 1) + 4 * (g & 1) + ((lane >> 2) & 3)) * 160 + (lane & 3) * 8;
  f32x4 acc[5][4] = {};
  bf16x8 of[4][4];
  k2_issue<0>(c, 0, of[0]); k2_issue<1>(c, 1, of[1]); k2_issue<2>(c, 2, of[2]); k2_issue<3>(c, 3, of[3]);
  for (int ks = 0; ks < 12; ks += 4) {
    k2_wait<27>(of[0]); k2_compute<0>(c, of[0], acc); k2_issue<0>(c, ks + 4, of[0]);
    k2_wait<27>(of[1]); k2_compute<1>(c, of[1], acc); k2_issue<1>(c, ks + 5, of[1]);
    k2_wait<27>(of[2]); k2_compute<2>(c, of[2], acc); k2_issue<2>(c, ks + 6, of[2]);
    k2_wait<27>(of[3]); k2_compute<3>(c, of[3], acc); k2_issue<3>(c, ks + 7, of[3]);
  }
  k2_wait<27>(of[0]); k2_compute<0>(c, of[0], acc);
  k2_wait<18>(of[1]); k2_compute<1>(c, of[1], acc);
  k2_wait<9>(of[2]); k2_compute<2>(c, of[2], acc);
  k2_wait<0>(of[3]); k2_compute<3>(c, of[3], acc);
  {
    char* slab = smem + w * K2_WAVE_B;
#pragma unroll
    for (int nt = 0; nt < 5; ++nt)
#pragma unroll
      for (int t = 0; t < 4; ++t) *(f32x4*)(slab + ((16 * t + r16) * 80 + 16 * nt + 4 * g) * 4) = acc[nt][t];
  }
  __syncthreads();
  for (int i = tid; i < 1280; i += 512) {
    f32x4 s = *(f32x4*)(smem + i * 16);
#pragma unroll
    for (int ww = 1; ww < 8; ++ww) s += *(f32x4*)(smem + ww * K2_WAVE_B + i * 16);
    const int q = i / 20, n4 = i % 20;
    u32x2 o2 = {cvtpk(s[0], s[1]), cvtpk(s[2], s[3])};
    *(u32x2*)(p.out + ((size_t)(mt * 64 + q) * 1280 + br * 640 + nt0 * 80 + n4 * 4) * 2) = o2;
  }
}

extern "C" void kernel_launch(void* const* d_in, const int* in_sizes, int n_in, void* d_out, int out_size, void* d_ws, size_t ws_size,
                              hipStream_t stream) {
  Params p;
  for (int i = 0; i < 2; ++i) {
    p.x[i] = (const char*)d_in[5 * i + 0]; p.k[i] = (const char*)d_in[5 * i + 1]; p.v[i] = (const char*)d_in[5 * i + 2];
    p.wq[i] = (const char*)d_in[5 * i + 3]; p.wo[i] = (const char*)d_in[5 * i + 4];
  }
  p.ows = (char*)d_ws; p.out = (char*)d_out;
  (void)hipFuncSetAttribute((const void*)attn_kernel, hipFuncAttributeMaxDynamicSharedMemorySize, NSLOT * SLOT);
  (void)hipFuncSetAttribute((const void*)oproj_kernel, hipFuncAttributeMaxDynamicSharedMemorySize, 8 * K2_WAVE_B);
  hipLaunchKernelGGL(attn_kernel, dim3(512), dim3(256), NSLOT * SLOT, stream, p);
  hipLaunchKernelGGL(oproj_kernel, dim3(256), dim3(512), 8 * K2_WAVE_B, stream, p);
}
```

```cpp
#include <hip/hip_runtime.h>
#include <hip/hip_bf16.h>
#include <type_traits>

typedef __bf16 bf16x8 __attribute__((ext_vector_type(8)));
typedef __bf16 bf16x4 __attribute__((ext_vector_type(4)));
typedef __bf16 bf16x2 __attribute__((ext_vector_type(2)));
typedef float f32x16 __attribute__((ext_vector_type(16)));
typedef float f32x2 __attribute__((ext_vector_type(2)));
typedef unsigned u32x4 __attribute__((ext_vector_type(4)));
typedef unsigned u32x2 __attribute__((ext_vector_type(2)));
typedef unsigned long long u64;
typedef __attribute__((address_space(1))) u64 gu64;
typedef __amdgpu_buffer_rsrc_t rsrc_t;
#define LAS __attribute__((address_space(3)))
#define DI __device__ __forceinline__
#define MFMA32(a, b, c) __builtin_amdgcn_mfma_f32_32x32x16_bf16((a), (b), (c), 0, 0, 0)
#define SB() __builtin_amdgcn_sched_barrier(0)
template <int V> using IC = std::integral_constant<int, V>;

constexpr int NB = 8, SQ = 128, DM = 640, NH = 32, SK = 512, DH = 128;
constexpr unsigned SLOT = 16384, RING = 5 * SLOT;
constexpr int NSLOT = 5;
constexpr float QSCALE = 0.088388347648318440f * 1.4426950408889634f;
constexpr int DLY = 4;
constexpr int WOTE = 12;
constexpr size_t WS_PART = 16u << 20;
constexpr size_t WS_FLAG = 37u << 20;

struct Params {
  const char* x[2]; const char* k[2]; const char* v[2]; const char* wq[2]; const char* wo[2];
  char* ows; char* out;
};

DI unsigned cvtpk(float lo, float hi) { f32x2 v = {lo, hi}; bf16x2 b = __builtin_convertvector(v, bf16x2); return __builtin_bit_cast(unsigned, b); }
DI bf16x8 pack8(const f32x16& a, int s, float sc) {
  u32x4 w = {cvtpk(a[8 * s] * sc, a[8 * s + 1] * sc), cvtpk(a[8 * s + 2] * sc, a[8 * s + 3] * sc),
             cvtpk(a[8 * s + 4] * sc, a[8 * s + 5] * sc), cvtpk(a[8 * s + 6] * sc, a[8 * s + 7] * sc)};
  return __builtin_bit_cast(bf16x8, w);
}
DI bf16x8 tr_frag(unsigned addr, int off0, int off1) {
  bf16x4 a = __builtin_amdgcn_ds_read_tr16_b64_v4bf16((LAS bf16x4*)(size_t)(addr + off0));
  bf16x4 b = __builtin_amdgcn_ds_read_tr16_b64_v4bf16((LAS bf16x4*)(size_t)(addr + off1));
  return __builtin_shufflevector(a, b, 0, 1, 2, 3, 4, 5, 6, 7);
}
DI bf16x8 lds_b128(unsigned addr) { return *(LAS bf16x8*)(size_t)addr; }
DI rsrc_t mk_rsrc(const void* p, unsigned bytes) { return __builtin_amdgcn_make_buffer_rsrc((void*)p, (short)0, (int)bytes, 0x00020000); }
template <int I, int E, class F> DI void sfor(F&& f) { if constexpr (I < E) { f(IC<I>{}); sfor<I + 1, E>(f); } }

DI void set_m0(unsigned a, unsigned b) { asm volatile("s_add_u32 m0, %0, %1\n\ts_nop 0" ::"s"(a), "s"(b) : "memory", "scc"); }
template <int IMM> DI void dma(unsigned voff, rsrc_t rs, unsigned soff) {
  asm volatile("buffer_load_dwordx4 %0, %1, %2 offen offset:%3 lds" ::"v"(voff), "s"(rs), "s"(soff), "i"(IMM) : "memory");
}
template <int IMM> DI void dma_nt(unsigned voff, rsrc_t rs, unsigned soff) {
  asm volatile("buffer_load_dwordx4 %0, %1, %2 offen offset:%3 nt lds" ::"v"(voff), "s"(rs), "s"(soff), "i"(IMM) : "memory");
}
template <int IMM> DI void ld_nt(u32x4& d, unsigned voff, rsrc_t rs, unsigned soff) {
  asm volatile("buffer_load_dwordx4 %0, %1, %2, %3 offen offset:%4 nt" : "=v"(d) : "v"(voff), "s"(rs), "s"(soff), "i"(IMM) : "memory");
}
template <int IMM> DI void ld_df(u32x4& d, unsigned voff, rsrc_t rs, unsigned soff) {
  asm volatile("buffer_load_dwordx4 %0, %1, %2, %3 offen offset:%4" : "=v"(d) : "v"(voff), "s"(rs), "s"(soff), "i"(IMM) : "memory");
}
template <int N> DI void wait_bar() {
  asm volatile("" ::: "memory");
  __builtin_amdgcn_s_waitcnt((N & 15) | (7 << 4) | ((N >> 4) << 14));
  __builtin_amdgcn_s_barrier();
  asm volatile("" ::: "memory");
}

struct Ctx {
  rsrc_t rk, rv, rw, rx;
  unsigned m0w;
  unsigned voffW[2], voffV, voffK[2], voffX[2];
  unsigned wbase, vbase, kbase[2], xbase[2];
  unsigned wr_base;
  unsigned rot;
};
constexpr unsigned XBIAS = 2048, VBIAS = 2048 + 16384;
DI unsigned rotc(const Ctx& c, int pos) { unsigned cc = (unsigned)pos + c.rot; return cc >= 10u ? cc - 10u : cc; }
template <int N, int P> DI void kv_piece(const Ctx& c) {
  constexpr int i = P & 1, IMM = P * 1024;
  if constexpr (N == 20) dma<IMM>(c.voffK[i], c.rk, P < 2 ? 0u : 8192u - 2048u);
  else if constexpr (P < 2) dma<IMM>(c.voffK[i], c.rk, (unsigned)(N - 19) * 8192u);
  else dma_nt<IMM>(c.voffV, c.rv, (unsigned)(N - 19) * 8192u);
}
template <int N> DI void item_m0(const Ctx& c) { set_m0(c.m0w, (N % NSLOT) * SLOT); }
template <int N, int i> DI void w_piece(const Ctx& c) { dma<i * 1024>(c.voffW[i], c.rw, (rotc(c, N >> 1) * 64u + 32u * (N & 1)) * 8192u); }
template <int C, int i> DI void x_piece(const Ctx& c) { dma<2048 + i * 1024>(c.voffX[i], c.rx, rotc(c, C) * 128u); }
DI bf16x8 rdW(unsigned wb, int i) { return tr_frag(wb, (i >> 2) * 8192 + (i & 3) * 256, (i >> 2) * 8192 + 1024 + (i & 3) * 256); }
DI bf16x8 rdX(const unsigned (&xb)[2], unsigned off, int s) { return lds_b128(xb[s & 1] + off + (s >> 1) * 512); }
DI bf16x8 rdK(const unsigned (&kb)[2], unsigned off, int s8) { return lds_b128(kb[s8 & 1] + off + (s8 >> 1) * 256); }
DI bf16x8 rdV(unsigned vb, int i) { return tr_frag(vb, (i >> 2) * 8192 + (i & 3) * 256, (i >> 2) * 8192 + 4096 + (i & 3) * 256); }

constexpr int NREG = 18, NPRE = 24;
struct Pre { int item, piece, reg, ep; };
constexpr Pre pre_piece(int j) {
  constexpr int it[24] = {20, 20, 21, 21, 20, 20, 22, 22, 23, 23, 21, 21, 24, 24, 25, 25, 25, 25, 22, 22, 23, 23, 24, 24};
  constexpr int pc[24] = {0, 1, 0, 1, 2, 3, 0, 1, 0, 1, 2, 3, 0, 1, 0, 1, 2, 3, 2, 3, 2, 3, 2, 3};
  constexpr int we[24] = {15, 15, 16, 16, 16, 16, 17, 17, 18, 18, 18, 18, 19, 19, 20, 20, 20, 20, 15, 15, 17, 17, 19, 19};
  return Pre{it[j], pc[j], j < NREG ? j : -1, we[j]};
}
constexpr int ep_piece(int M, int E, int K) {
  int k = 0;
  for (int j = 0; j < NPRE; ++j) { const Pre q = pre_piece(j); if (q.ep == E && (M == 0 || q.reg >= 0)) { if (k == K) return j; ++k; } }
  return -1;
}
struct Sched { int n; int need[256]; int before[40]; };
constexpr Sched make_sched(int M) {
  Sched s{}; int n = 0;
  auto add = [&](int need) { s.need[n++] = need; };
  auto kvp = [&](int e, int k) { if (M == 0) { const int j = ep_piece(0, e, k); if (j >= 0) add(pre_piece(j).item - 1); } };
  add(-1); add(-1); add(-1); add(-1); add(0); add(0); add(1); add(1); add(1); add(1); add(2); add(2); add(3); add(3);
  s.before[0] = n;
  for (int e = 0; e < 36; ++e) {
    s.before[e + 1] = n;
    if (e < 20) {
      if (e + 5 < 20) add(e + 4);
      kvp(e, 0);
      if (e + 5 < 20) add(e + 4);
      kvp(e, 1);
      if (e == WOTE) add(19);
      if ((e & 1) == 0 && e / 2 + 2 <= 9) add(e + 3);
      kvp(e, 2);
      if ((e & 1) == 0 && e / 2 + 2 <= 9) add(e + 3);
      kvp(e, 3);
    } else if (e == 20) { kvp(e, 0); kvp(e, 1); kvp(e, 2); kvp(e, 3); }
    else if (e <= 31) { add(e + 4); add(e + 4); add(e + 4); add(e + 4); }
  }
  s.n = n;
  return s;
}
template <int M> struct SC { static constexpr Sched S = make_sched(M); };
template <int M> constexpr int bar_n(int e) {
  const int issued = SC<M>::S.before[e + 1]; int last = -1;
  for (int i = 0; i < issued; ++i) if (SC<M>::S.need[i] <= e) last = i;
  return issued - 1 - last;
}
template <int M> constexpr bool sched_ok() {
  for (int e = -1; e < 35; ++e) for (int i = SC<M>::S.before[e + 1]; i < SC<M>::S.n; ++i) if (SC<M>::S.need[i] <= e) return false;
  for (int e = 25; e <= 32; ++e) if (bar_n<M>(e) != 12) return false;
  return bar_n<M>(33) == 8 && bar_n<M>(34) == 4;
}
static_assert(sched_ok<0>() && sched_ok<1>(), "ring schedule");

template <int J> DI void pre_load(const Ctx& c, u32x4 (&kv)[NREG]) {
  constexpr Pre q = pre_piece(J); constexpr int N = q.item, P = q.piece, i = P & 1, IMM = P * 1024;
  if constexpr (q.reg < 0) { item_m0<N>(c); kv_piece<N, P>(c); }
  else if constexpr (N == 20 || P < 2) ld_df<IMM>(kv[q.reg], c.voffK[i], c.rk, N == 20 ? (P < 2 ? 0u : 8192u - 2048u) : (unsigned)(N - 19) * 8192u);
  else ld_nt<IMM>(kv[q.reg], c.voffV, c.rv, (unsigned)(N - 19) * 8192u);
}
template <int M, int E, int K> DI void pre_place(const Ctx& c, u32x4 (&kv)[NREG]) {
  constexpr int j = ep_piece(M, E, K);
  if constexpr (j >= 0) {
    constexpr Pre q = pre_piece(j);
    if constexpr (M == 1) *(LAS u32x4*)(size_t)(c.wr_base + (q.item % NSLOT) * SLOT + q.piece * 1024) = kv[q.reg];
    else { item_m0<q.item>(c); kv_piece<q.item, q.piece>(c); }
  }
}
template <int M, int Z, class F>
DI void qstep(const Ctx& c, f32x16 (&qa)[4], bf16x8 (&pf)[4], bf16x8 (&xc)[4], bf16x8 (&xn)[4], u32x4 (&kv)[NREG], F&& touch) {
  constexpr unsigned slot = (Z % NSLOT) * SLOT, nslot = ((Z + 1) % NSLOT) * SLOT;
  bf16x8 wr[8];
#pragma unroll
  for (int i = 0; i < 4; ++i) wr[i] = pf[i];
  sfor<0, 8>([&](auto ic) {
    constexpr int i = decltype(ic)::value;
    if constexpr (Z >= 1) {
      constexpr int e = Z - 1;
      if constexpr ((e & 1) == 0 && e / 2 + 2 <= 9) {
        if constexpr (i == 1) { set_m0(c.m0w, ((e / 2) & 1) * SLOT); x_piece<e / 2 + 2, 0>(c); }
        if constexpr (i == 3) x_piece<e / 2 + 2, 1>(c);
      }
      if constexpr (i == 1) pre_place<M, e, 2>(c, kv);
      if constexpr (i == 3) pre_place<M, e, 3>(c, kv);
    }
    if constexpr (i == 5) { if constexpr (Z + 5 < 20) w_piece<Z + 5, 0>(c); pre_place<M, Z, 0>(c, kv); }
    if constexpr (i == 7) { if constexpr (Z + 5 < 20) w_piece<Z + 5, 1>(c); pre_place<M, Z, 1>(c, kv); if constexpr (Z == WOTE) touch(); }
    if constexpr (i < 2) { wr[5 + 2 * i] = rdW(c.wbase + slot, 5 + 2 * i); wr[4 + 2 * i] = rdW(c.wbase + slot, 4 + 2 * i); }
    if constexpr (i == 4) { wait_bar<bar_n<M>(Z)>(); if constexpr (Z + 5 < 20) item_m0<Z + 5>(c); }
    if constexpr (i >= 4) {
      constexpr int f = (i - 4) ^ 1;
      if constexpr ((Z & 1) && Z < 19) xn[i - 4] = rdX(c.xbase, (((Z + 1) >> 1) & 1) * SLOT, i - 4);
      if constexpr (Z < 19) pf[f] = rdW(c.wbase + nslot, f); else pf[f] = rdK(c.kbase, nslot, f);
    }
    SB();
    qa[i & 3] = MFMA32(wr[i], xc[2 * (Z & 1) + (i >> 2)], qa[i & 3]);
    SB();
  });
}

struct Att {
  f32x16 o[4], negm;
  bf16x8 qf[8];
  float l;
};
DI float rowmax(const f32x16& s) {
  float mx = s[0];
#pragma unroll
  for (int i = 1; i < 16; ++i) mx = fmaxf(mx, s[i]);
  auto rr = __builtin_amdgcn_permlane32_swap(__float_as_uint(mx), __float_as_uint(mx), false, false);
  return fmaxf(__uint_as_float(rr[0]), __uint_as_float(rr[1]));
}
struct Bases { unsigned k[2], v; };
DI Bases bases(const Ctx& c, unsigned slot) {
  Bases b; b.k[0] = c.kbase[0] + slot; b.k[1] = c.kbase[1] + slot; b.v = c.vbase + slot;
  asm volatile("" : "+v"(b.k[0]), "+v"(b.k[1]), "+v"(b.v));
  return b;
}
struct NoHook { template <class T> DI void operator()(T) const {} };
template <bool QK, bool EX, bool NQK, bool NPV, bool FIRST, bool FILL, int NW, class H = NoHook>
DI void astep(const Ctx& c, Att& a, unsigned soff, unsigned slot, unsigned nslot, Bases& bs, bf16x8 (&pf)[4], f32x16& sc, f32x16& sn,
              bf16x8 (&pp)[2], bf16x8 (&pn)[2], H hook = H{}) {
  constexpr int NM = QK ? 16 : 8, BARP = NM - 4;
  bf16x8 fr[NM];
#pragma unroll
  for (int i = 0; i < 4; ++i) fr[i] = pf[i];
  float e[16]; unsigned pw[8];
  sfor<0, NM>([&](auto ic) {
    constexpr int i = decltype(ic)::value;
    if constexpr (QK) {
      if constexpr (i < 2) { fr[5 + 2 * i] = rdK(bs.k, 0, 5 + 2 * i); fr[4 + 2 * i] = rdK(bs.k, 0, 4 + 2 * i); }
      else if constexpr (i < 10) { constexpr int f = (i - 2) ^ 1; fr[8 + f] = rdV(bs.v, f); }
    } else {
      if constexpr (i < 2) { fr[5 + 2 * i] = rdV(bs.v, 5 + 2 * i); fr[4 + 2 * i] = rdV(bs.v, 4 + 2 * i); }
    }
    if constexpr (NW >= 0) {
      if constexpr (i == BARP - 1) bs = bases(c, nslot);
      if constexpr (i == BARP) { wait_bar<NW>(); if constexpr (FILL) set_m0(c.m0w, slot); }
      if constexpr (i >= BARP) {
        constexpr int f = (i - BARP) ^ 1;
        if constexpr (NQK) pf[f] = rdK(bs.k, 0, f); else if constexpr (NPV) pf[f] = rdV(bs.v, f);
      }
    }
    SB();
    if constexpr (QK && i < 8) sn = MFMA32(fr[i], a.qf[i], i == 0 ? a.negm : sn);
    else {
      constexpr int j = i - (QK ? 8 : 0);
      if constexpr (FIRST && j < 4) a.o[j] = MFMA32(fr[i], pp[0], f32x16{}); else a.o[j & 3] = MFMA32(fr[i], pp[j >> 2], a.o[j & 3]);
    }
    SB();
    if constexpr (QK && i == 12) __builtin_amdgcn_s_setprio(1);
    if constexpr (QK && i == 4) __builtin_amdgcn_s_setprio(0);
    if constexpr (!FILL && i >= BARP) { hook(IC<i - BARP>{}); SB(); }
    if constexpr (FILL && i >= BARP) {
      constexpr int P = i - BARP;
      if constexpr (P < 2) dma<P * 1024>(c.voffK[P], c.rk, soff); else dma_nt<P * 1024>(c.voffV, c.rv, soff);
      SB();
    }
    if constexpr (EX) {
#pragma unroll
      for (int k = i * 16 / NM; k < (i + 1) * 16 / NM; ++k) {
        e[k] = __builtin_amdgcn_exp2f(sc[k]);
        if (k >= 1) a.l += e[k - 1];
        if (k >= 2 && (k & 1) == 0) pw[(k - 2) >> 1] = cvtpk(e[k - 2], e[k - 1]);
      }
    }
  });
  if constexpr (EX) {
    a.l += e[15]; pw[7] = cvtpk(e[14], e[15]);
    pn[0] = __builtin_bit_cast(bf16x8, (u32x4){pw[0], pw[1], pw[2], pw[3]});
    pn[1] = __builtin_bit_cast(bf16x8, (u32x4){pw[4], pw[5], pw[6], pw[7]});
  }
}
DI void attn_safe(const Ctx& c, Att& a) {
  float m = -3.0e38f, l = 0.f;
#pragma unroll
  for (int dt = 0; dt < 4; ++dt) a.o[dt] = f32x16{};
  for (int t = 0; t < 16; ++t) {
    wait_bar<0>();
    set_m0(c.m0w, 0u);
    dma<0>(c.voffK[0], c.rk, (unsigned)t * 8192u); dma<1024>(c.voffK[1], c.rk, (unsigned)t * 8192u);
    dma_nt<2048>(c.voffV, c.rv, (unsigned)(t + 2) * 8192u); dma_nt<3072>(c.voffV, c.rv, (unsigned)(t + 2) * 8192u);
    wait_bar<0>();
    f32x16 s = {};
#pragma unroll
    for (int i = 0; i < 8; ++i) s = MFMA32(rdK(c.kbase, 0, i), a.qf[i], s);
    const float mn = fmaxf(m, rowmax(s)), f = __builtin_amdgcn_exp2f(m - mn);
    m = mn;
    float sum = 0.f;
#pragma unroll
    for (int i = 0; i < 16; ++i) { s[i] = __builtin_amdgcn_exp2f(s[i] - m); sum += s[i]; }
    l = l * f + sum;
    const bf16x8 p[2] = {pack8(s, 0, 1.0f), pack8(s, 1, 1.0f)};
#pragma unroll
    for (int dt = 0; dt < 4; ++dt)
#pragma unroll
      for (int i = 0; i < 16; ++i) a.o[dt][i] *= f;
#pragma unroll
    for (int i = 0; i < 8; ++i) a.o[i & 3] = MFMA32(rdV(c.vbase, i), p[i >> 2], a.o[i & 3]);
  }
  a.l = l;
}
DI void pack_o(const Att& a, bf16x8 (&oa)[4], bf16x8 (&ob)[4]) {
  float l = a.l;
  auto rr = __builtin_amdgcn_permlane32_swap(__float_as_uint(l), __float_as_uint(l), false, false);
  l = __uint_as_float(rr[0]) + __uint_as_float(rr[1]);
  const float il = __builtin_amdgcn_rcpf(l);
#pragma unroll
  for (int f = 0; f < 4; ++f) { oa[f] = pack8(a.o[f >> 1], f & 1, il); ob[f] = pack8(a.o[2 + (f >> 1)], f & 1, il); }
}
DI unsigned nxt(unsigned s) { return s == (NSLOT - 1) * SLOT ? 0u : s + SLOT; }

constexpr unsigned K2SLOT = 20480, WOBIAS = 4096;
struct Ctx2 { rsrc_t ro, rw; unsigned m0w, voffO, voffW[5], tbase, pair; };
DI unsigned k64(const Ctx2& c, int s) { return 4u * ((c.pair + (unsigned)(s >> 1)) & 3u) + (unsigned)(s & 1); }
template <int IMM> DI void oload(bf16x8& d, unsigned voff, rsrc_t rs, unsigned soff) {
  asm volatile("buffer_load_dwordx4 %0, %1, %2, %3 offen offset:%4 sc1" : "=v"(d) : "v"(voff), "s"(rs), "s"(soff), "i"(IMM) : "memory");
}
DI void oload4(const Ctx2& c, int s, bf16x8 (&o)[4]) {
  const unsigned so = k64(c, s) * 4096u;
  oload<0>(o[0], c.voffO, c.ro, so); oload<1024>(o[1], c.voffO, c.ro, so); oload<2048>(o[2], c.voffO, c.ro, so); oload<3072>(o[3], c.voffO, c.ro, so);
}
template <int J, int P> DI void wo_piece(const Ctx2& c) {
  constexpr unsigned slot = ((J + 2) & 3) * K2SLOT;
  if constexpr (P == 0) set_m0(c.m0w, slot);
  if constexpr (P == 4) set_m0(c.m0w, slot + 4096);
  dma<(P & 3) * 1024>(c.voffW[P], c.rw, k64(c, J) * (64u * DM * 2));
}
DI bf16x8 rdWo(unsigned tb, int i) { return tr_frag(tb, (i / 5) * 5120 + (i % 5) * 256, (i / 5) * 5120 + 2560 + (i % 5) * 256); }
template <int NW> DI void wait_bar_o(bf16x8 (&on)[4]) {
  asm volatile("" ::: "memory");
  __builtin_amdgcn_s_waitcnt((NW & 15) | (7 << 4) | ((NW >> 4) << 14));
  __builtin_amdgcn_s_barrier();
  asm volatile("" : "+v"(on[0]), "+v"(on[1]), "+v"(on[2]), "+v"(on[3])::"memory");
}
template <int s, int NW, bool FO, class F>
DI void k2step(const Ctx2& c, f32x16 (&acc)[5], bf16x8 (&pf)[4], bf16x8 (&oc)[4], bf16x8 (&op)[4], bf16x8 (&on)[4], F&& after_bar) {
  constexpr unsigned slot = ((s + 2) & 3) * K2SLOT, nslot = ((s + 3) & 3) * K2SLOT;
  bf16x8 fr[20];
#pragma unroll
  for (int i = 0; i < 4; ++i) fr[i] = pf[i];
  sfor<0, 20>([&](auto ic) {
    constexpr int i = decltype(ic)::value;
    if constexpr (i < 2) { fr[5 + 2 * i] = rdWo(c.tbase + slot, 5 + 2 * i); fr[4 + 2 * i] = rdWo(c.tbase + slot, 4 + 2 * i); }
    else if constexpr (i < 14) { constexpr int f = 8 + ((i - 2) ^ 1); fr[f] = rdWo(c.tbase + slot, f); }
    if constexpr (NW >= 0) {
      if constexpr (i == 16) { wait_bar_o<NW>(on); after_bar(); }
      if constexpr (i >= 16) { constexpr int f = (i - 16) ^ 1; pf[f] = rdWo(c.tbase + nslot, f); }
    }
    SB();
    acc[i % 5] = MFMA32(fr[i], oc[i / 5], acc[i % 5]);
    SB();
    if constexpr (FO && (i == 1 || i == 3 || i == 5 || i == 7)) { oload<(i >> 1) * 1024>(op[i >> 1], c.voffO, c.ro, k64(c, s + 3) * 4096u); SB(); }
    if constexpr (s == 0) {
      if constexpr (i == 16) { wo_piece<3, 3>(c); wo_piece<3, 4>(c); SB(); }
      if constexpr (i >= 17) { wo_piece<4, i - 17>(c); SB(); }
    } else {
      if constexpr (s + 3 < 8 && (i == 9 || i == 11)) { wo_piece<s + 3, 3 + ((i - 9) >> 1)>(c); SB(); }
      if constexpr (s + 4 < 8 && i >= 16 && i < 19) { wo_piece<s + 4, i - 16>(c); SB(); }
    }
  });
}
template <int NF> DI void wait_flags(const u64* f, u64 tag, unsigned lflag, int w, int lane) {
  for (int it = 0; it < (1 << 15); ++it) {
    if (w == 0) {
      const u64 v = __hip_atomic_load((gu64*)(f + (lane & (NF - 1))), __ATOMIC_RELAXED, __HIP_MEMORY_SCOPE_AGENT);
      const int ok = __all(v == tag);
      if (lane == 0) *(LAS int*)(size_t)(lflag + 4 * (it & 1)) = ok;
    }
    wait_bar<63>();
    const int ok = __builtin_amdgcn_readfirstlane(*(LAS int*)(size_t)(lflag + 4 * (it & 1)));
    if (ok) break;
  }
}

template <int M> DI void body(const Params& p) {
  extern __shared__ __attribute__((aligned(16))) char smem[];
  int tid = threadIdx.x;
  asm volatile("" : "+v"(tid));
  const int lane = tid & 63;
  const int w8 = __builtin_amdgcn_readfirstlane(tid >> 6), w = w8 & 3, team = w8 >> 2;
  const int r = lane & 31, h = lane >> 5;
  const int bid = blockIdx.x, grp = bid & 7, idx = bid >> 3;
  const int br = grp >> 2, hg = grp & 3, pair = idx >> 3, b = idx & 7, head = hg * 8 + pair * 2 + team;
  u64* const flags = (u64*)(p.ows + WS_FLAG);
  u64 epoch;
  asm volatile("global_load_dwordx2 %0, %1, %2 sc1" : "=v"(epoch) : "v"(0u), "s"(flags) : "memory");
  Ctx c;
  c.rot = (unsigned)(b + ((5 * pair) >> 1)) % 10u;
  c.rk = mk_rsrc(p.k[br] + (size_t)(b * NH + head) * (SK * DH * 2), SK * DH * 2);
  c.rv = mk_rsrc(p.v[br] + (size_t)(b * NH + head) * (SK * DH * 2) - VBIAS, SK * DH * 2 + VBIAS);
  c.rw = mk_rsrc(p.wq[br] + head * 256, DM * 8192 - head * 256);
  c.rx = mk_rsrc(p.x[br] + (size_t)b * (SQ * DM * 2) - XBIAS, SQ * DM * 2 + XBIAS);
  const unsigned lds0 = (unsigned)(size_t)(LAS char*)smem, lds = lds0 + team * RING;
  c.m0w = lds + w * 4096;
  c.wr_base = lds + w * 4096 + lane * 16;
  const int q4 = (lane >> 2) & 3, p4 = lane & 3, g1 = (lane >> 4) & 1;
  c.voffV = (8 * w + q4) * 256 + (lane >> 4) * 64 + p4 * 16;
#pragma unroll
  for (int i = 0; i < 2; ++i) {
    c.voffW[i] = (8 * w + 4 * i + q4) * 8192 + (lane >> 4) * 64 + p4 * 16 - 1024 * i;
    c.voffK[i] = (8 * w + q4) * 256 + (lane >> 4) * 64 + ((p4 ^ ((2 * w + i) & 3)) * 16);
    const int r8 = (lane >> 2) & 7;
    c.voffX[i] = (64 * team + 16 * w + 8 * i + r8) * (DM * 2) + h * 64 + ((p4 ^ ((2 * i + (r8 >> 2)) & 3)) * 16) - 1024 * i;
    c.kbase[i] = lds + (r >> 3) * 4096 + ((r >> 2) & 1) * 1024 + (r & 3) * 64 + (((2 * i + h) ^ ((r >> 2) & 3)) * 16);
    const int xn = 4 * (w & 1) + (r >> 3);
    c.xbase[i] = lds0 + (w >> 1) * RING + (xn >> 1) * 4096 + (xn & 1) * 1024 + 2048 + (r & 7) * 64 + (((2 * i + h) ^ ((r >> 2) & 3)) * 16);
  }
  const int p4s = ((p4 & 1) << 1) | (p4 >> 1);
  c.wbase = lds + h * 4096 + q4 * 64 + g1 * 32 + p4s * 8;
  c.vbase = lds + h * 1024 + 2048 + q4 * 64 + g1 * 32 + p4 * 8;

  u32x4 kv[NREG];
  if constexpr (M == 1) sfor<0, NPRE>([&](auto j) { pre_load<decltype(j)::value>(c, kv); });
  item_m0<0>(c); w_piece<0, 0>(c); w_piece<0, 1>(c); x_piece<0, 0>(c); x_piece<0, 1>(c);
  item_m0<1>(c); w_piece<1, 0>(c); w_piece<1, 1>(c);
  item_m0<2>(c); w_piece<2, 0>(c); w_piece<2, 1>(c);
  item_m0<1>(c); x_piece<1, 0>(c); x_piece<1, 1>(c);
  item_m0<3>(c); w_piece<3, 0>(c); w_piece<3, 1>(c);
  item_m0<4>(c); w_piece<4, 0>(c); w_piece<4, 1>(c);
  f32x16 qa[4] = {};
  bf16x8 pf[4], xa[4], xb[4];
  unsigned dmy = 0;
  auto touch = [&]() {
    const rsrc_t rwo = mk_rsrc(p.wo[br] + (size_t)hg * 1024 * (DM * 2), 1024 * DM * 2);
    const unsigned off = tid < 320 ? (unsigned)(idx * 320 + tid) * 128u : 0x40000000u;
    asm volatile("buffer_load_dword %0, %1, %2, 0 offen" : "=v"(dmy) : "v"(off), "s"(rwo) : "memory");
  };
  wait_bar<bar_n<M>(-1)>();
  asm volatile("" : "+v"(epoch));
  const u64 tag = (((u64)(unsigned)__builtin_amdgcn_readfirstlane((int)(epoch >> 32)) << 32) | (unsigned)__builtin_amdgcn_readfirstlane((int)epoch)) + 1;
#pragma unroll
  for (int s = 0; s < 4; ++s) xa[s] = rdX(c.xbase, 0u, s);
#pragma unroll
  for (int k = 0; k < 4; ++k) pf[k] = rdW(c.wbase, k);
  sfor<0, 5>([&](auto zc) {
    constexpr int z = 4 * decltype(zc)::value;
    qstep<M, z>(c, qa, pf, xa, xb, kv, touch); qstep<M, z + 1>(c, qa, pf, xa, xb, kv, touch);
    qstep<M, z + 2>(c, qa, pf, xb, xa, kv, touch); qstep<M, z + 3>(c, qa, pf, xb, xa, kv, touch);
  });
  asm volatile("" ::"v"(dmy));

  Att a;
#pragma unroll
  for (int s8 = 0; s8 < 8; ++s8) a.qf[s8] = pack8(qa[s8 >> 1], s8 & 1, QSCALE);
  f32x16 s0 = {}, s1 = {};
  bf16x8 pa[2], pb[2];
  {
    constexpr unsigned slot = (20 % NSLOT) * SLOT, nslot = (21 % NSLOT) * SLOT;
    bf16x8 fr[16];
#pragma unroll
    for (int i = 0; i < 4; ++i) fr[i] = pf[i];
    float m = 0.f, e[16]; unsigned pw[8];
    sfor<0, 16>([&](auto ic) {
      constexpr int i = decltype(ic)::value;
      if constexpr (i == 1) pre_place<M, 19, 2>(c, kv);
      if constexpr (i == 3) pre_place<M, 19, 3>(c, kv);
      if constexpr (i < 6) { constexpr int f = 4 + 2 * i; fr[f + 1] = rdK(c.kbase, slot + ((f + 1) >> 3) * 2048, (f + 1) & 7); fr[f] = rdK(c.kbase, slot + (f >> 3) * 2048, f & 7); }
      if constexpr (i == 12) wait_bar<bar_n<M>(20)>();
      if constexpr (i >= 12) { constexpr int f = (i - 12) ^ 1; pf[f] = rdK(c.kbase, nslot, f); }
      SB();
      if constexpr (i < 8) s0 = MFMA32(fr[i], a.qf[i], s0); else s1 = MFMA32(fr[i], a.qf[i - 8], s1);
      SB();
      if constexpr (i >= 12) { pre_place<M, 20, i - 12>(c, kv); SB(); }
      if constexpr (i == 9) m = rowmax(s0);
      if constexpr (i >= 10) {
#pragma unroll
        for (int k = (i - 10) * 16 / 6; k < (i - 9) * 16 / 6; ++k) { e[k] = __builtin_amdgcn_exp2f(s0[k] - m); a.negm[k] = -m; }
      }
    });
    float sum = 0.f;
#pragma unroll
    for (int k = 0; k < 8; ++k) { sum += e[2 * k] + e[2 * k + 1]; pw[k] = cvtpk(e[2 * k], e[2 * k + 1]); }
    a.l = sum;
    pa[0] = __builtin_bit_cast(bf16x8, (u32x4){pw[0], pw[1], pw[2], pw[3]});
    pa[1] = __builtin_bit_cast(bf16x8, (u32x4){pw[4], pw[5], pw[6], pw[7]});
#pragma unroll
    for (int i = 0; i < 16; ++i) s1[i] -= m;
  }
  if (team) for (int i = 0; i < DLY; ++i) __builtin_amdgcn_s_barrier();

  unsigned slot = (21 % NSLOT) * SLOT, soff = 7 * 8192;
  Bases bs = bases(c, slot);
  {
    unsigned ns = nxt(slot);
    astep<true, true, true, true, true, true, bar_n<M>(21)>(c, a, soff, slot, ns, bs, pf, s1, s0, pa, pb);
    slot = ns; ns = nxt(slot);
    astep<true, true, true, true, false, true, bar_n<M>(22)>(c, a, soff + 8192, slot, ns, bs, pf, s0, s1, pb, pa);
    slot = ns; soff += 16384; ns = nxt(slot);
    astep<true, true, true, true, false, true, bar_n<M>(23)>(c, a, soff, slot, ns, bs, pf, s1, s0, pa, pb);
    slot = ns; ns = nxt(slot);
    astep<true, true, true, true, false, true, bar_n<M>(24)>(c, a, soff + 8192, slot, ns, bs, pf, s0, s1, pb, pa);
    slot = ns; soff += 16384;
  }
  for (int u = 4; u < 10; u += 2) {
    unsigned ns = nxt(slot);
    astep<true, true, true, true, false, true, 12>(c, a, soff, slot, ns, bs, pf, s1, s0, pa, pb);
    slot = ns; ns = nxt(slot);
    astep<true, true, true, true, false, true, 12>(c, a, soff + 8192, slot, ns, bs, pf, s0, s1, pb, pa);
    slot = ns; soff += 16384;
  }
  const int nq = pair, cl = ((grp * NB + b) * 2 + team) * 4;
  Ctx2 d;
  {
    unsigned ns = nxt(slot);
    astep<true, true, true, true, false, true, 12>(c, a, soff, slot, ns, bs, pf, s1, s0, pa, pb);
    slot = ns; ns = nxt(slot);
    astep<true, true, true, true, false, false, 12>(c, a, soff, slot, ns, bs, pf, s0, s1, pb, pa);
    slot = ns; ns = nxt(slot);
    d.pair = pair;
    d.rw = mk_rsrc(p.wo[br] + (size_t)(hg * 1024 + team * 128 + 16 * w) * (DM * 2) + nq * 320 - WOBIAS, (1024 - team * 128 - 16 * w) * (DM * 2) - nq * 320 + WOBIAS);
    d.m0w = lds + w * 5120;
#pragma unroll
    for (int P = 0; P < 5; ++P) {
      const int blk = 4 * P + (lane >> 4), kq = blk / 5, nb = blk % 5;
      d.voffW[P] = (4 * kq + q4) * (DM * 2) + nb * 64 + p4 * 16 + WOBIAS - (P & 3) * 1024;
    }
    astep<true, true, true, true, false, false, 8>(c, a, soff, slot, ns, bs, pf, s1, s0, pa, pb, [&](auto pc) {
      constexpr int P = decltype(pc)::value; wo_piece<0, P>(d); if constexpr (P == 3) wo_piece<0, 4>(d); });
    slot = ns; ns = nxt(slot);
    astep<true, true, false, true, false, false, 9>(c, a, soff, slot, ns, bs, pf, s0, s1, pb, pa, [&](auto pc) {
      constexpr int P = decltype(pc)::value; wo_piece<1, P>(d); if constexpr (P == 3) wo_piece<1, 4>(d); });
    slot = ns; ns = nxt(slot);
    astep<false, true, false, true, false, false, 10>(c, a, soff, slot, ns, bs, pf, s1, s0, pa, pb);
    slot = ns; ns = nxt(slot);
    astep<false, false, false, false, false, false, -1>(c, a, soff, slot, ns, bs, pf, s0, s1, pb, pa);
  }
  d.ro = mk_rsrc(p.ows + ((size_t)((br * NB + b) * 4 + w) * NH + 8 * hg + team) * 8192, 65536 - team * 8192);
  d.voffO = lane * 16;
  d.tbase = lds + h * 1280 + q4 * 64 + g1 * 32 + p4 * 8;
  bf16x8 o0[4], o1[4], o2[4], o3[4];
  auto put_o = [&]() {
    pack_o(a, o0, o1);
#pragma unroll
    for (int f = 0; f < 4; ++f) {
      __builtin_amdgcn_raw_buffer_store_b128(__builtin_bit_cast(u32x4, o0[f]), d.ro, d.voffO + f * 1024, pair * 16384, 16);
      __builtin_amdgcn_raw_buffer_store_b128(__builtin_bit_cast(u32x4, o1[f]), d.ro, d.voffO + 4096 + f * 1024, pair * 16384, 16);
    }
  };
  put_o();
  {
    const unsigned gw = lds + K2SLOT + 3 * 5120 + 4096;
    float big = a.l;
#pragma unroll
    for (int dt = 0; dt < 4; ++dt)
#pragma unroll
      for (int i = 0; i < 16; i += 2)
        big = __builtin_elementwise_maximum(big, __builtin_elementwise_maximum(fabsf(a.o[dt][i]), fabsf(a.o[dt][i + 1])));
    const bool bad = __any(!(big < 1.0e36f));
    if (lane == 0) *(LAS int*)(size_t)(gw + 4 * w) = bad;
    wait_bar<8>();
    const u32x4 fl = *(LAS u32x4*)(size_t)gw;
    if (__builtin_expect(__builtin_amdgcn_readfirstlane(fl[0] | fl[1] | fl[2] | fl[3]), 0)) { attn_safe(c, a); put_o(); wait_bar<0>(); }
  }

#pragma unroll
  for (int k = 0; k < 4; ++k) pf[k] = rdWo(d.tbase + 2 * K2SLOT, k);
  sfor<0, 5>([&](auto P) { wo_piece<2, decltype(P)::value>(d); });
  sfor<0, 3>([&](auto P) { wo_piece<3, decltype(P)::value>(d); });
  auto publish = [&] { if (w == 0 && lane == 0) __hip_atomic_store((gu64*)(flags + 64 + cl + pair), tag, __ATOMIC_RELAXED, __HIP_MEMORY_SCOPE_AGENT); };
  if (team == 0) {
    __builtin_amdgcn_s_barrier();
    __builtin_amdgcn_s_barrier();
    asm volatile("s_waitcnt vmcnt(0)\n\ts_barrier" ::: "memory");
    publish();
  }
  f32x16 acc[5] = {};
  auto nop = [] {};
  k2step<0, 0, false>(d, acc, pf, o0, o3, o1, publish);
  k2step<1, 63, false>(d, acc, pf, o1, o0, o2, nop);
  wait_flags<4>(flags + 64 + cl, tag, lds + 3 * K2SLOT + 4096, w, lane);
  oload4(d, 2, o2); oload4(d, 3, o3); oload4(d, 4, o0);
  wait_bar_o<8>(o2);
  k2step<2, 10, true>(d, acc, pf, o2, o1, o3, nop);
  k2step<3, 15, true>(d, acc, pf, o3, o2, o0, nop);
  k2step<4, 18, true>(d, acc, pf, o0, o3, o1, nop);
  k2step<5, 9, false>(d, acc, pf, o1, o0, o2, nop);
  k2step<6, 0, false>(d, acc, pf, o2, o1, o3, nop);
  k2step<7, -1, false>(d, acc, pf, o3, o2, o0, nop);

  const rsrc_t rp = mk_rsrc(p.ows + WS_PART, 512 * 40960);
  const int tl8 = ((br * NB + b) * 4 + nq) * 8;
  {
    const unsigned pw_ = (unsigned)(((tl8 + hg * 2 + team) * 4 + w) * 10240) + lane * 16;
#pragma unroll
    for (int f = 0; f < 10; ++f) __builtin_amdgcn_raw_buffer_store_b128(__builtin_bit_cast(u32x4, pack8(acc[f >> 1], f & 1, 1.0f)), rp, pw_ + f * 1024, 0, 16);
    asm volatile("s_waitcnt vmcnt(0)" ::: "memory");
    wait_bar<0>();
    if (w == 0 && lane == 0) __hip_atomic_store((gu64*)(flags + 576 + tl8 + hg * 2 + team), tag, __ATOMIC_RELAXED, __HIP_MEMORY_SCOPE_AGENT);
  }
  if (team) {
    wait_flags<8>(flags + 576 + tl8, tag, lds, w, lane);
    u32x4 v[3][8];
#pragma unroll
    for (int k = 0; k < 3; ++k)
#pragma unroll
      for (int g2 = 0; g2 < 8; ++g2) {
        const int f = w + 4 * k;
        v[k][g2] = __builtin_amdgcn_raw_buffer_load_b128(rp, lane * 16 + (f < 10 ? 0u : 0x40000000u), (unsigned)(((tl8 + g2) * 4 + hg) * 10240 + f * 1024), 16);
      }
#pragma unroll
    for (int k = 0; k < 3; ++k) {
      const int f = w + 4 * k;
      float sm[8] = {};
#pragma unroll
      for (int g2 = 0; g2 < 8; ++g2)
#pragma unroll
        for (int e = 0; e < 4; ++e) { sm[2 * e] += __uint_as_float(v[k][g2][e] << 16); sm[2 * e + 1] += __uint_as_float(v[k][g2][e] & 0xffff0000u); }
      if (f < 10) {
        const int q = b * 128 + hg * 32 + r, n = br * 640 + nq * 160 + 16 * f + 4 * h;
        char* o = p.out + ((size_t)q * 1280 + n) * 2;
        *(u32x2*)o = (u32x2){cvtpk(sm[0], sm[1]), cvtpk(sm[2], sm[3])};
        *(u32x2*)(o + 16) = (u32x2){cvtpk(sm[4], sm[5]), cvtpk(sm[6], sm[7])};
      }
    }
    if (bid == 0 && w == 1) {
      for (int it = 0; it < (1 << 15); ++it) {
        bool ok = true;
#pragma unroll
        for (int k = 0; k < 8; ++k) ok &= __hip_atomic_load((gu64*)(flags + 64 + k * 64 + lane), __ATOMIC_RELAXED, __HIP_MEMORY_SCOPE_AGENT) == tag;
        if (__all(ok)) break;
      }
      if (lane == 0) __hip_atomic_store((gu64*)flags, tag, __ATOMIC_RELAXED, __HIP_MEMORY_SCOPE_AGENT);
    }
  }
}

__global__ __launch_bounds__(512, 2) void fused_attn_kernel(Params p) {
  if ((blockIdx.x >> 3) & 1) body<1>(p); else body<0>(p);
}

extern "C" void kernel_launch(void* const* d_in, const int* in_sizes, int n_in, void* d_out, int out_size, void* d_ws, size_t ws_size,
                              hipStream_t stream) {
  Params p;
  for (int i = 0; i < 2; ++i) {
    p.x[i] = (const char*)d_in[5 * i + 0]; p.k[i] = (const char*)d_in[5 * i + 1]; p.v[i] = (const char*)d_in[5 * i + 2];
    p.wq[i] = (const char*)d_in[5 * i + 3]; p.wo[i] = (const char*)d_in[5 * i + 4];
  }
  p.ows = (char*)d_ws;
  p.out = (char*)d_out;
  (void)hipFuncSetAttribute((const void*)fused_attn_kernel, hipFuncAttributeMaxDynamicSharedMemorySize, 2 * RING);
  hipLaunchKernelGGL(fused_attn_kernel, dim3(256), dim3(512), 2 * RING, stream, p);
}
```

```cpp
#include <hip/hip_runtime.h>
#include <hip/hip_bf16.h>
#include <type_traits>

typedef __bf16 bf16x8 __attribute__((ext_vector_type(8)));
typedef __bf16 bf16x4 __attribute__((ext_vector_type(4)));
typedef __bf16 bf16x2 __attribute__((ext_vector_type(2)));
typedef float f32x16 __attribute__((ext_vector_type(16)));
typedef float f32x2 __attribute__((ext_vector_type(2)));
typedef unsigned u32x4 __attribute__((ext_vector_type(4)));
typedef unsigned u32x2 __attribute__((ext_vector_type(2)));
typedef unsigned long long u64;
typedef __attribute__((address_space(1))) u64 gu64;
typedef __amdgpu_buffer_rsrc_t rsrc_t;
#define LAS __attribute__((address_space(3)))
#define DI __device__ __forceinline__
#define MFMA32(a, b, c) __builtin_amdgcn_mfma_f32_32x32x16_bf16((a), (b), (c), 0, 0, 0)
#define SB() __builtin_amdgcn_sched_barrier(0)
template <int V> using IC = std::integral_constant<int, V>;

constexpr int NB = 8, SQ = 128, DM = 640, NH = 32, SK = 512, DH = 128;
constexpr unsigned SLOT = 16384, RING = 5 * SLOT;
constexpr int NSLOT = 5;
constexpr float QSCALE = 0.088388347648318440f * 1.4426950408889634f;
constexpr int DLY = 4;
constexpr int WOTE = 12;
constexpr size_t WS_PART = 16u << 20;
constexpr size_t WS_FLAG = 37u << 20;

struct Params {
  const char* x[2]; const char* k[2]; const char* v[2]; const char* wq[2]; const char* wo[2];
  char* ows; char* out;
};

DI unsigned cvtpk(float lo, float hi) { f32x2 v = {lo, hi}; bf16x2 b = __builtin_convertvector(v, bf16x2); return __builtin_bit_cast(unsigned, b); }
DI bf16x8 pack8(const f32x16& a, int s, float sc) {
  u32x4 w = {cvtpk(a[8 * s] * sc, a[8 * s + 1] * sc), cvtpk(a[8 * s + 2] * sc, a[8 * s + 3] * sc),
             cvtpk(a[8 * s + 4] * sc, a[8 * s + 5] * sc), cvtpk(a[8 * s + 6] * sc, a[8 * s + 7] * sc)};
  return __builtin_bit_cast(bf16x8, w);
}
DI bf16x8 tr_frag(unsigned addr, int off0, int off1) {
  bf16x4 a = __builtin_amdgcn_ds_read_tr16_b64_v4bf16((LAS bf16x4*)(size_t)(addr + off0));
  bf16x4 b = __builtin_amdgcn_ds_read_tr16_b64_v4bf16((LAS bf16x4*)(size_t)(addr + off1));
  return __builtin_shufflevector(a, b, 0, 1, 2, 3, 4, 5, 6, 7);
}
DI bf16x8 lds_b128(unsigned addr) { return *(LAS bf16x8*)(size_t)addr; }
DI rsrc_t mk_rsrc(const void* p, unsigned bytes) { return __builtin_amdgcn_make_buffer_rsrc((void*)p, (short)0, (int)bytes, 0x00020000); }
template <int I, int E, class F> DI void sfor(F&& f) { if constexpr (I < E) { f(IC<I>{}); sfor<I + 1, E>(f); } }

DI void set_m0(unsigned a, unsigned b) { asm volatile("s_add_u32 m0, %0, %1\n\ts_nop 0" ::"s"(a), "s"(b) : "memory", "scc"); }
template <int IMM> DI void dma(unsigned voff, rsrc_t rs, unsigned soff) {
  asm volatile("buffer_load_dwordx4 %0, %1, %2 offen offset:%3 lds" ::"v"(voff), "s"(rs), "s"(soff), "i"(IMM) : "memory");
}
template <int IMM> DI void dma_nt(unsigned voff, rsrc_t rs, unsigned soff) {
  asm volatile("buffer_load_dwordx4 %0, %1, %2 offen offset:%3 nt lds" ::"v"(voff), "s"(rs), "s"(soff), "i"(IMM) : "memory");
}
template <int IMM> DI void ld_nt(u32x4& d, unsigned voff, rsrc_t rs, unsigned soff) {
  asm volatile("buffer_load_dwordx4 %0, %1, %2, %3 offen offset:%4 nt" : "=v"(d) : "v"(voff), "s"(rs), "s"(soff), "i"(IMM) : "memory");
}
template <int N> DI void wait_bar() {
  asm volatile("" ::: "memory");
  __builtin_amdgcn_s_waitcnt((N & 15) | (7 << 4) | ((N >> 4) << 14));
  __builtin_amdgcn_s_barrier();
  asm volatile("" ::: "memory");
}

struct Ctx {
  rsrc_t rk, rv, rw, rx;
  unsigned m0w;
  unsigned voffW[2], voffV, voffK[2], voffX[2];
  unsigned wbase, vbase, kbase[2], xbase[2];
  unsigned wr_base;
  unsigned rot;
};
constexpr unsigned XBIAS = 2048, VBIAS = 2048 + 16384;
DI unsigned rotc(const Ctx& c, int pos) { unsigned cc = (unsigned)pos + c.rot; return cc >= 10u ? cc - 10u : cc; }
template <int N, int P> DI void kv_piece(const Ctx& c) {
  constexpr int i = P & 1, IMM = P * 1024;
  if constexpr (N == 20) dma_nt<IMM>(c.voffK[i], c.rk, P < 2 ? 0u : 8192u - 2048u);
  else if constexpr (P < 2) dma_nt<IMM>(c.voffK[i], c.rk, (unsigned)(N - 19) * 8192u);
  else dma_nt<IMM>(c.voffV, c.rv, (unsigned)(N - 19) * 8192u);
}
template <int N> DI void item_m0(const Ctx& c) { set_m0(c.m0w, (N % NSLOT) * SLOT); }
template <int N, int i> DI void w_piece(const Ctx& c) { dma<i * 1024>(c.voffW[i], c.rw, (rotc(c, N >> 1) * 64u + 32u * (N & 1)) * 8192u); }
template <int C, int i> DI void x_piece(const Ctx& c) { dma<2048 + i * 1024>(c.voffX[i], c.rx, rotc(c, C) * 128u); }
DI bf16x8 rdW(unsigned wb, int i) { return tr_frag(wb, (i >> 2) * 8192 + (i & 3) * 256, (i >> 2) * 8192 + 1024 + (i & 3) * 256); }
DI bf16x8 rdX(const unsigned (&xb)[2], unsigned off, int s) { return lds_b128(xb[s & 1] + off + (s >> 1) * 512); }
DI bf16x8 rdK(const unsigned (&kb)[2], unsigned off, int s8) { return lds_b128(kb[s8 & 1] + off + (s8 >> 1) * 256); }
DI bf16x8 rdV(unsigned vb, int i) { return tr_frag(vb, (i >> 2) * 8192 + (i & 3) * 256, (i >> 2) * 8192 + 4096 + (i & 3) * 256); }

constexpr int NREG = 18, NPRE = 24;
struct Pre { int item, piece, reg, ep; };
constexpr Pre pre_piece(int j) {
  constexpr int it[24] = {20, 20, 21, 21, 20, 20, 22, 22, 23, 23, 21, 21, 24, 24, 25, 25, 25, 25, 22, 22, 23, 23, 24, 24};
  constexpr int pc[24] = {0, 1, 0, 1, 2, 3, 0, 1, 0, 1, 2, 3, 0, 1, 0, 1, 2, 3, 2, 3, 2, 3, 2, 3};
  constexpr int we[24] = {15, 15, 16, 16, 16, 16, 17, 17, 18, 18, 18, 18, 19, 19, 20, 20, 20, 20, 15, 15, 17, 17, 19, 19};
  return Pre{it[j], pc[j], j < NREG ? j : -1, we[j]};
}
constexpr int ep_piece(int M, int E, int K) {
  int k = 0;
  for (int j = 0; j < NPRE; ++j) { const Pre q = pre_piece(j); if (q.ep == E && (M == 0 || q.reg >= 0)) { if (k == K) return j; ++k; } }
  return -1;
}
struct Sched { int n; int need[256]; int before[40]; };
constexpr Sched make_sched(int M) {
  Sched s{}; int n = 0;
  auto add = [&](int need) { s.need[n++] = need; };
  auto kvp = [&](int e, int k) { if (M == 0) { const int j = ep_piece(0, e, k); if (j >= 0) add(pre_piece(j).item - 1); } };
  add(-1); add(-1); add(-1); add(-1); add(0); add(0); add(1); add(1); add(1); add(1); add(2); add(2); add(3); add(3);
  s.before[0] = n;
  for (int e = 0; e < 36; ++e) {
    s.before[e + 1] = n;
    if (e < 20) {
      if (e + 5 < 20) add(e + 4);
      kvp(e, 0);
      if (e + 5 < 20) add(e + 4);
      kvp(e, 1);
      if (e == WOTE) add(19);
      if ((e & 1) == 0 && e / 2 + 2 <= 9) add(e + 3);
      kvp(e, 2);
      if ((e & 1) == 0 && e / 2 + 2 <= 9) add(e + 3);
      kvp(e, 3);
    } else if (e == 20) { kvp(e, 0); kvp(e, 1); kvp(e, 2); kvp(e, 3); }
    else if (e <= 31) { add(e + 4); add(e + 4); add(e + 4); add(e + 4); }
  }
  s.n = n;
  return s;
}
template <int M> struct SC { static constexpr Sched S = make_sched(M); };
template <int M> constexpr int bar_n(int e) {
  const int issued = SC<M>::S.before[e + 1]; int last = -1;
  for (int i = 0; i < issued; ++i) if (SC<M>::S.need[i] <= e) last = i;
  return issued - 1 - last;
}
template <int M> constexpr bool sched_ok() {
  for (int e = -1; e < 35; ++e) for (int i = SC<M>::S.before[e + 1]; i < SC<M>::S.n; ++i) if (SC<M>::S.need[i] <= e) return false;
  for (int e = 25; e <= 32; ++e) if (bar_n<M>(e) != 12) return false;
  return bar_n<M>(33) == 8 && bar_n<M>(34) == 4;
}
static_assert(sched_ok<0>() && sched_ok<1>(), "ring schedule");

template <int J> DI void pre_load(const Ctx& c, u32x4 (&kv)[NREG]) {
  constexpr Pre q = pre_piece(J); constexpr int N = q.item, P = q.piece, i = P & 1, IMM = P * 1024;
  if constexpr (q.reg < 0) { item_m0<N>(c); kv_piece<N, P>(c); }
  else if constexpr (N == 20 || P < 2) ld_nt<IMM>(kv[q.reg], c.voffK[i], c.rk, N == 20 ? (P < 2 ? 0u : 8192u - 2048u) : (unsigned)(N - 19) * 8192u);
  else ld_nt<IMM>(kv[q.reg], c.voffV, c.rv, (unsigned)(N - 19) * 8192u);
}
template <int M, int E, int K> DI void pre_place(const Ctx& c, u32x4 (&kv)[NREG]) {
  constexpr int j = ep_piece(M, E, K);
  if constexpr (j >= 0) {
    constexpr Pre q = pre_piece(j);
    if constexpr (M == 1) *(LAS u32x4*)(size_t)(c.wr_base + (q.item % NSLOT) * SLOT + q.piece * 1024) = kv[q.reg];
    else { item_m0<q.item>(c); kv_piece<q.item, q.piece>(c); }
  }
}
template <int M, int Z, class F>
DI void qstep(const Ctx& c, f32x16 (&qa)[4], bf16x8 (&pf)[4], bf16x8 (&xc)[4], bf16x8 (&xn)[4], u32x4 (&kv)[NREG], F&& touch) {
  constexpr unsigned slot = (Z % NSLOT) * SLOT, nslot = ((Z + 1) % NSLOT) * SLOT;
  bf16x8 wr[8];
#pragma unroll
  for (int i = 0; i < 4; ++i) wr[i] = pf[i];
  sfor<0, 8>([&](auto ic) {
    constexpr int i = decltype(ic)::value;
    if constexpr (Z >= 1) {
      constexpr int e = Z - 1;
      if constexpr ((e & 1) == 0 && e / 2 + 2 <= 9) {
        if constexpr (i == 1) { set_m0(c.m0w, ((e / 2) & 1) * SLOT); x_piece<e / 2 + 2, 0>(c); }
        if constexpr (i == 3) x_piece<e / 2 + 2, 1>(c);
      }
      if constexpr (i == 1) pre_place<M, e, 2>(c, kv);
      if constexpr (i == 3) pre_place<M, e, 3>(c, kv);
    }
    if constexpr (i == 5) { if constexpr (Z + 5 < 20) w_piece<Z + 5, 0>(c); pre_place<M, Z, 0>(c, kv); }
    if constexpr (i == 7) { if constexpr (Z + 5 < 20) w_piece<Z + 5, 1>(c); pre_place<M, Z, 1>(c, kv); if constexpr (Z == WOTE) touch(); }
    if constexpr (i < 2) { wr[5 + 2 * i] = rdW(c.wbase + slot, 5 + 2 * i); wr[4 + 2 * i] = rdW(c.wbase + slot, 4 + 2 * i); }
    if constexpr (i == 4) { wait_bar<bar_n<M>(Z)>(); if constexpr (Z + 5 < 20) item_m0<Z + 5>(c); }
    if constexpr (i >= 4) {
      constexpr int f = (i - 4) ^ 1;
      if constexpr ((Z & 1) && Z < 19) xn[i - 4] = rdX(c.xbase, (((Z + 1) >> 1) & 1) * SLOT, i - 4);
      if constexpr (Z < 19) pf[f] = rdW(c.wbase + nslot, f); else pf[f] = rdK(c.kbase, nslot, f);
    }
    SB();
    qa[i & 3] = MFMA32(wr[i], xc[2 * (Z & 1) + (i >> 2)], qa[i & 3]);
    SB();
  });
}

struct Att {
  f32x16 o[4], negm;
  bf16x8 qf[8];
  float l;
};
DI float rowmax(const f32x16& s) {
  float mx = s[0];
#pragma unroll
  for (int i = 1; i < 16; ++i) mx = fmaxf(mx, s[i]);
  auto rr = __builtin_amdgcn_permlane32_swap(__float_as_uint(mx), __float_as_uint(mx), false, false);
  return fmaxf(__uint_as_float(rr[0]), __uint_as_float(rr[1]));
}
struct Bases { unsigned k[2], v; };
DI Bases bases(const Ctx& c, unsigned slot) {
  Bases b; b.k[0] = c.kbase[0] + slot; b.k[1] = c.kbase[1] + slot; b.v = c.vbase + slot;
  asm volatile("" : "+v"(b.k[0]), "+v"(b.k[1]), "+v"(b.v));
  return b;
}
struct NoHook { template <class T> DI void operator()(T) const {} };
template <bool QK, bool EX, bool NQK, bool NPV, bool FIRST, bool FILL, int NW, class H = NoHook>
DI void astep(const Ctx& c, Att& a, unsigned soff, unsigned slot, unsigned nslot, Bases& bs, bf16x8 (&pf)[4], f32x16& sc, f32x16& sn,
              bf16x8 (&pp)[2], bf16x8 (&pn)[2], H hook = H{}) {
  constexpr int NM = QK ? 16 : 8, BARP = NM - 4;
  bf16x8 fr[NM];
#pragma unroll
  for (int i = 0; i < 4; ++i) fr[i] = pf[i];
  float e[16]; unsigned pw[8];
  sfor<0, NM>([&](auto ic) {
    constexpr int i = decltype(ic)::value;
    if constexpr (QK) {
      if constexpr (i < 2) { fr[5 + 2 * i] = rdK(bs.k, 0, 5 + 2 * i); fr[4 + 2 * i] = rdK(bs.k, 0, 4 + 2 * i); }
      else if constexpr (i < 10) { constexpr int f = (i - 2) ^ 1; fr[8 + f] = rdV(bs.v, f); }
    } else {
      if constexpr (i < 2) { fr[5 + 2 * i] = rdV(bs.v, 5 + 2 * i); fr[4 + 2 * i] = rdV(bs.v, 4 + 2 * i); }
    }
    if constexpr (NW >= 0) {
      if constexpr (i == BARP - 1) bs = bases(c, nslot);
      if constexpr (i == BARP) { wait_bar<NW>(); if constexpr (FILL) set_m0(c.m0w, slot); }
      if constexpr (i >= BARP) {
        constexpr int f = (i - BARP) ^ 1;
        if constexpr (NQK) pf[f] = rdK(bs.k, 0, f); else if constexpr (NPV) pf[f] = rdV(bs.v, f);
      }
    }
    SB();
    if constexpr (QK && i < 8) sn = MFMA32(fr[i], a.qf[i], i == 0 ? a.negm : sn);
    else {
      constexpr int j = i - (QK ? 8 : 0);
      if constexpr (FIRST && j < 4) a.o[j] = MFMA32(fr[i], pp[0], f32x16{}); else a.o[j & 3] = MFMA32(fr[i], pp[j >> 2], a.o[j & 3]);
    }
    SB();
    if constexpr (QK && i == 12) __builtin_amdgcn_s_setprio(1);
    if constexpr (QK && i == 4) __builtin_amdgcn_s_setprio(0);
    if constexpr (!FILL && i >= BARP) { hook(IC<i - BARP>{}); SB(); }
    if constexpr (FILL && i >= BARP) {
      constexpr int P = i - BARP;
      if constexpr (P < 2) dma_nt<P * 1024>(c.voffK[P], c.rk, soff); else dma_nt<P * 1024>(c.voffV, c.rv, soff);
      SB();
    }
    if constexpr (EX) {
#pragma unroll
      for (int k = i * 16 / NM; k < (i + 1) * 16 / NM; ++k) {
        e[k] = __builtin_amdgcn_exp2f(sc[k]);
        if (k >= 1) a.l += e[k - 1];
        if (k >= 2 && (k & 1) == 0) pw[(k - 2) >> 1] = cvtpk(e[k - 2], e[k - 1]);
      }
    }
  });
  if constexpr (EX) {
    a.l += e[15]; pw[7] = cvtpk(e[14], e[15]);
    pn[0] = __builtin_bit_cast(bf16x8, (u32x4){pw[0], pw[1], pw[2], pw[3]});
    pn[1] = __builtin_bit_cast(bf16x8, (u32x4){pw[4], pw[5], pw[6], pw[7]});
  }
}
DI void attn_safe(const Ctx& c, Att& a) {
  float m = -3.0e38f, l = 0.f;
#pragma unroll
  for (int dt = 0; dt < 4; ++dt) a.o[dt] = f32x16{};
  for (int t = 0; t < 16; ++t) {
    wait_bar<0>();
    set_m0(c.m0w, 0u);
    dma_nt<0>(c.voffK[0], c.rk, (unsigned)t * 8192u); dma_nt<1024>(c.voffK[1], c.rk, (unsigned)t * 8192u);
    dma_nt<2048>(c.voffV, c.rv, (unsigned)(t + 2) * 8192u); dma_nt<3072>(c.voffV, c.rv, (unsigned)(t + 2) * 8192u);
    wait_bar<0>();
    f32x16 s = {};
#pragma unroll
    for (int i = 0; i < 8; ++i) s = MFMA32(rdK(c.kbase, 0, i), a.qf[i], s);
    const float mn = fmaxf(m, rowmax(s)), f = __builtin_amdgcn_exp2f(m - mn);
    m = mn;
    float sum = 0.f;
#pragma unroll
    for (int i = 0; i < 16; ++i) { s[i] = __builtin_amdgcn_exp2f(s[i] - m); sum += s[i]; }
    l = l * f + sum;
    const bf16x8 p[2] = {pack8(s, 0, 1.0f), pack8(s, 1, 1.0f)};
#pragma unroll
    for (int dt = 0; dt < 4; ++dt)
#pragma unroll
      for (int i = 0; i < 16; ++i) a.o[dt][i] *= f;
#pragma unroll
    for (int i = 0; i < 8; ++i) a.o[i & 3] = MFMA32(rdV(c.vbase, i), p[i >> 2], a.o[i & 3]);
  }
  a.l = l;
}
DI void pack_o(const Att& a, bf16x8 (&oa)[4], bf16x8 (&ob)[4]) {
  float l = a.l;
  auto rr = __builtin_amdgcn_permlane32_swap(__float_as_uint(l), __float_as_uint(l), false, false);
  l = __uint_as_float(rr[0]) + __uint_as_float(rr[1]);
  const float il = __builtin_amdgcn_rcpf(l);
#pragma unroll
  for (int f = 0; f < 4; ++f) { oa[f] = pack8(a.o[f >> 1], f & 1, il); ob[f] = pack8(a.o[2 + (f >> 1)], f & 1, il); }
}
DI unsigned nxt(unsigned s) { return s == (NSLOT - 1) * SLOT ? 0u : s + SLOT; }

constexpr unsigned K2SLOT = 20480, WOBIAS = 4096;
struct Ctx2 { rsrc_t ro, rw; unsigned m0w, voffO, voffW[5], tbase, pair; };
DI unsigned k64(const Ctx2& c, int s) { return 4u * ((c.pair + (unsigned)(s >> 1)) & 3u) + (unsigned)(s & 1); }
template <int IMM> DI void oload(bf16x8& d, unsigned voff, rsrc_t rs, unsigned soff) {
  asm volatile("buffer_load_dwordx4 %0, %1, %2, %3 offen offset:%4 sc1" : "=v"(d) : "v"(voff), "s"(rs), "s"(soff), "i"(IMM) : "memory");
}
DI void oload4(const Ctx2& c, int s, bf16x8 (&o)[4]) {
  const unsigned so = k64(c, s) * 4096u;
  oload<0>(o[0], c.voffO, c.ro, so); oload<1024>(o[1], c.voffO, c.ro, so); oload<2048>(o[2], c.voffO, c.ro, so); oload<3072>(o[3], c.voffO, c.ro, so);
}
template <int J, int P> DI void wo_piece(const Ctx2& c) {
  constexpr unsigned slot = ((J + 2) & 3) * K2SLOT;
  if constexpr (P == 0) set_m0(c.m0w, slot);
  if constexpr (P == 4) set_m0(c.m0w, slot + 4096);
  dma<(P & 3) * 1024>(c.voffW[P], c.rw, k64(c, J) * (64u * DM * 2));
}
DI bf16x8 rdWo(unsigned tb, int i) { return tr_frag(tb, (i / 5) * 5120 + (i % 5) * 256, (i / 5) * 5120 + 2560 + (i % 5) * 256); }
template <int NW> DI void wait_bar_o(bf16x8 (&on)[4]) {
  asm volatile("" ::: "memory");
  __builtin_amdgcn_s_waitcnt((NW & 15) | (7 << 4) | ((NW >> 4) << 14));
  __builtin_amdgcn_s_barrier();
  asm volatile("" : "+v"(on[0]), "+v"(on[1]), "+v"(on[2]), "+v"(on[3])::"memory");
}
template <int s, int NW, bool FO, class F>
DI void k2step(const Ctx2& c, f32x16 (&acc)[5], bf16x8 (&pf)[4], bf16x8 (&oc)[4], bf16x8 (&op)[4], bf16x8 (&on)[4], F&& after_bar) {
  constexpr unsigned slot = ((s + 2) & 3) * K2SLOT, nslot = ((s + 3) & 3) * K2SLOT;
  bf16x8 fr[20];
#pragma unroll
  for (int i = 0; i < 4; ++i) fr[i] = pf[i];
  sfor<0, 20>([&](auto ic) {
    constexpr int i = decltype(ic)::value;
    if constexpr (i < 2) { fr[5 + 2 * i] = rdWo(c.tbase + slot, 5 + 2 * i); fr[4 + 2 * i] = rdWo(c.tbase + slot, 4 + 2 * i); }
    else if constexpr (i < 14) { constexpr int f = 8 + ((i - 2) ^ 1); fr[f] = rdWo(c.tbase + slot, f); }
    if constexpr (NW >= 0) {
      if constexpr (i == 16) { wait_bar_o<NW>(on); after_bar(); }
      if constexpr (i >= 16) { constexpr int f = (i - 16) ^ 1; pf[f] = rdWo(c.tbase + nslot, f); }
    }
    SB();
    acc[i % 5] = MFMA32(fr[i], oc[i / 5], acc[i % 5]);
    SB();
    if constexpr (FO && (i == 1 || i == 3 || i == 5 || i == 7)) { oload<(i >> 1) * 1024>(op[i >> 1], c.voffO, c.ro, k64(c, s + 3) * 4096u); SB(); }
    if constexpr (s == 0) {
      if constexpr (i == 16) { wo_piece<3, 3>(c); wo_piece<3, 4>(c); SB(); }
      if constexpr (i >= 17) { wo_piece<4, i - 17>(c); SB(); }
    } else {
      if constexpr (s + 3 < 8 && (i == 9 || i == 11)) { wo_piece<s + 3, 3 + ((i - 9) >> 1)>(c); SB(); }
      if constexpr (s + 4 < 8 && i >= 16 && i < 19) { wo_piece<s + 4, i - 16>(c); SB(); }
    }
  });
}
template <int NF> DI void wait_flags(const u64* f, u64 tag, unsigned lflag, int w, int lane) {
  for (int it = 0; it < (1 << 15); ++it) {
    if (w == 0) {
      const u64 v = __hip_atomic_load((gu64*)(f + (lane & (NF - 1))), __ATOMIC_RELAXED, __HIP_MEMORY_SCOPE_AGENT);
      const int ok = __all(v == tag);
      if (lane == 0) *(LAS int*)(size_t)(lflag + 4 * (it & 1)) = ok;
    }
    wait_bar<63>();
    const int ok = __builtin_amdgcn_readfirstlane(*(LAS int*)(size_t)(lflag + 4 * (it & 1)));
    if (ok) break;
  }
}

template <int M> DI void body(const Params& p) {
  extern __shared__ __attribute__((aligned(16))) char smem[];
  int tid = threadIdx.x;
  asm volatile("" : "+v"(tid));
  const int lane = tid & 63;
  const int w8 = __builtin_amdgcn_readfirstlane(tid >> 6), w = w8 & 3, team = w8 >> 2;
  const int r = lane & 31, h = lane >> 5;
  const int bid = blockIdx.x, grp = bid & 7, idx = bid >> 3;
  const int br = grp >> 2, hg = grp & 3, pair = idx >> 3, b = idx & 7, head = hg * 8 + pair * 2 + team;
  u64* const flags = (u64*)(p.ows + WS_FLAG);
  u64 epoch;
  asm volatile("global_load_dwordx2 %0, %1, %2 sc1" : "=v"(epoch) : "v"(0u), "s"(flags) : "memory");
  Ctx c;
  c.rot = (unsigned)(b + ((5 * pair) >> 1)) % 10u;
  c.rk = mk_rsrc(p.k[br] + (size_t)(b * NH + head) * (SK * DH * 2), SK * DH * 2);
  c.rv = mk_rsrc(p.v[br] + (size_t)(b * NH + head) * (SK * DH * 2) - VBIAS, SK * DH * 2 + VBIAS);
  c.rw = mk_rsrc(p.wq[br] + head * 256, DM * 8192 - head * 256);
  c.rx = mk_rsrc(p.x[br] + (size_t)b * (SQ * DM * 2) - XBIAS, SQ * DM * 2 + XBIAS);
  const unsigned lds0 = (unsigned)(size_t)(LAS char*)smem, lds = lds0 + team * RING;
  c.m0w = lds + w * 4096;
  c.wr_base = lds + w * 4096 + lane * 16;
  const int q4 = (lane >> 2) & 3, p4 = lane & 3, g1 = (lane >> 4) & 1;
  c.voffV = (8 * w + q4) * 256 + (lane >> 4) * 64 + p4 * 16;
#pragma unroll
  for (int i = 0; i < 2; ++i) {
    c.voffW[i] = (8 * w + 4 * i + q4) * 8192 + (lane >> 4) * 64 + p4 * 16 - 1024 * i;
    c.voffK[i] = (8 * w + q4) * 256 + (lane >> 4) * 64 + ((p4 ^ ((2 * w + i) & 3)) * 16);
    const int r8 = (lane >> 2) & 7;
    c.voffX[i] = (64 * team + 16 * w + 8 * i + r8) * (DM * 2) + h * 64 + ((p4 ^ ((2 * i + (r8 >> 2)) & 3)) * 16) - 1024 * i;
    c.kbase[i] = lds + (r >> 3) * 4096 + ((r >> 2) & 1) * 1024 + (r & 3) * 64 + (((2 * i + h) ^ ((r >> 2) & 3)) * 16);
    const int xn = 4 * (w & 1) + (r >> 3);
    c.xbase[i] = lds0 + (w >> 1) * RING + (xn >> 1) * 4096 + (xn & 1) * 1024 + 2048 + (r & 7) * 64 + (((2 * i + h) ^ ((r >> 2) & 3)) * 16);
  }
  const int p4s = ((p4 & 1) << 1) | (p4 >> 1);
  c.wbase = lds + h * 4096 + q4 * 64 + g1 * 32 + p4s * 8;
  c.vbase = lds + h * 1024 + 2048 + q4 * 64 + g1 * 32 + p4 * 8;

  u32x4 kv[NREG];
  if constexpr (M == 1) sfor<0, NPRE>([&](auto j) { pre_load<decltype(j)::value>(c, kv); });
  item_m0<0>(c); w_piece<0, 0>(c); w_piece<0, 1>(c); x_piece<0, 0>(c); x_piece<0, 1>(c);
  item_m0<1>(c); w_piece<1, 0>(c); w_piece<1, 1>(c);
  item_m0<2>(c); w_piece<2, 0>(c); w_piece<2, 1>(c);
  item_m0<1>(c); x_piece<1, 0>(c); x_piece<1, 1>(c);
  item_m0<3>(c); w_piece<3, 0>(c); w_piece<3, 1>(c);
  item_m0<4>(c); w_piece<4, 0>(c); w_piece<4, 1>(c);
  f32x16 qa[4] = {};
  bf16x8 pf[4], xa[4], xb[4];
  unsigned dmy = 0;
  auto touch = [&]() {
    const rsrc_t rwo = mk_rsrc(p.wo[br] + (size_t)hg * 1024 * (DM * 2), 1024 * DM * 2);
    const unsigned off = tid < 320 ? (unsigned)(idx * 320 + tid) * 128u : 0x40000000u;
    asm volatile("buffer_load_dword %0, %1, %2, 0 offen" : "=v"(dmy) : "v"(off), "s"(rwo) : "memory");
  };
  wait_bar<bar_n<M>(-1)>();
  asm volatile("" : "+v"(epoch));
  const u64 tag = (((u64)(unsigned)__builtin_amdgcn_readfirstlane((int)(epoch >> 32)) << 32) | (unsigned)__builtin_amdgcn_readfirstlane((int)epoch)) + 1;
#pragma unroll
  for (int s = 0; s < 4; ++s) xa[s] = rdX(c.xbase, 0u, s);
#pragma unroll
  for (int k = 0; k < 4; ++k) pf[k] = rdW(c.wbase, k);
  sfor<0, 5>([&](auto zc) {
    constexpr int z = 4 * decltype(zc)::value;
    qstep<M, z>(c, qa, pf, xa, xb, kv, touch); qstep<M, z + 1>(c, qa, pf, xa, xb, kv, touch);
    qstep<M, z + 2>(c, qa, pf, xb, xa, kv, touch); qstep<M, z + 3>(c, qa, pf, xb, xa, kv, touch);
  });
  asm volatile("" ::"v"(dmy));

  Att a;
#pragma unroll
  for (int s8 = 0; s8 < 8; ++s8) a.qf[s8] = pack8(qa[s8 >> 1], s8 & 1, QSCALE);
  f32x16 s0 = {}, s1 = {};
  bf16x8 pa[2], pb[2];
  {
    constexpr unsigned slot = (20 % NSLOT) * SLOT, nslot = (21 % NSLOT) * SLOT;
    bf16x8 fr[16];
#pragma unroll
    for (int i = 0; i < 4; ++i) fr[i] = pf[i];
    float m = 0.f, e[16]; unsigned pw[8];
    sfor<0, 16>([&](auto ic) {
      constexpr int i = decltype(ic)::value;
      if constexpr (i == 1) pre_place<M, 19, 2>(c, kv);
      if constexpr (i == 3) pre_place<M, 19, 3>(c, kv);
      if constexpr (i < 6) { constexpr int f = 4 + 2 * i; fr[f + 1] = rdK(c.kbase, slot + ((f + 1) >> 3) * 2048, (f + 1) & 7); fr[f] = rdK(c.kbase, slot + (f >> 3) * 2048, f & 7); }
      if constexpr (i == 12) wait_bar<bar_n<M>(20)>();
      if constexpr (i >= 12) { constexpr int f = (i - 12) ^ 1; pf[f] = rdK(c.kbase, nslot, f); }
      SB();
      if constexpr (i < 8) s0 = MFMA32(fr[i], a.qf[i], s0); else s1 = MFMA32(fr[i], a.qf[i - 8], s1);
      SB();
      if constexpr (i >= 12) { pre_place<M, 20, i - 12>(c, kv); SB(); }
      if constexpr (i == 9) m = rowmax(s0);
      if constexpr (i >= 10) {
#pragma unroll
        for (int k = (i - 10) * 16 / 6; k < (i - 9) * 16 / 6; ++k) { e[k] = __builtin_amdgcn_exp2f(s0[k] - m); a.negm[k] = -m; }
      }
    });
    float sum = 0.f;
#pragma unroll
    for (int k = 0; k < 8; ++k) { sum += e[2 * k] + e[2 * k + 1]; pw[k] = cvtpk(e[2 * k], e[2 * k + 1]); }
    a.l = sum;
    pa[0] = __builtin_bit_cast(bf16x8, (u32x4){pw[0], pw[1], pw[2], pw[3]});
    pa[1] = __builtin_bit_cast(bf16x8, (u32x4){pw[4], pw[5], pw[6], pw[7]});
#pragma unroll
    for (int i = 0; i < 16; ++i) s1[i] -= m;
  }
  if (team) for (int i = 0; i < DLY; ++i) __builtin_amdgcn_s_barrier();

  unsigned slot = (21 % NSLOT) * SLOT, soff = 7 * 8192;
  Bases bs = bases(c, slot);
  {
    unsigned ns = nxt(slot);
    astep<true, true, true, true, true, true, bar_n<M>(21)>(c, a, soff, slot, ns, bs, pf, s1, s0, pa, pb);
    slot = ns; ns = nxt(slot);
    astep<true, true, true, true, false, true, bar_n<M>(22)>(c, a, soff + 8192, slot, ns, bs, pf, s0, s1, pb, pa);
    slot = ns; soff += 16384; ns = nxt(slot);
    astep<true, true, true, true, false, true, bar_n<M>(23)>(c, a, soff, slot, ns, bs, pf, s1, s0, pa, pb);
    slot = ns; ns = nxt(slot);
    astep<true, true, true, true, false, true, bar_n<M>(24)>(c, a, soff + 8192, slot, ns, bs, pf, s0, s1, pb, pa);
    slot = ns; soff += 16384;
  }
  for (int u = 4; u < 10; u += 2) {
    unsigned ns = nxt(slot);
    astep<true, true, true, true, false, true, 12>(c, a, soff, slot, ns, bs, pf, s1, s0, pa, pb);
    slot = ns; ns = nxt(slot);
    astep<true, true, true, true, false, true, 12>(c, a, soff + 8192, slot, ns, bs, pf, s0, s1, pb, pa);
    slot = ns; soff += 16384;
  }
  const int nq = pair, cl = ((grp * NB + b) * 2 + team) * 4;
  Ctx2 d;
  {
    unsigned ns = nxt(slot);
    astep<true, true, true, true, false, true, 12>(c, a, soff, slot, ns, bs, pf, s1, s0, pa, pb);
    slot = ns; ns = nxt(slot);
    astep<true, true, true, true, false, false, 12>(c, a, soff, slot, ns, bs, pf, s0, s1, pb, pa);
    slot = ns; ns = nxt(slot);
    d.pair = pair;
    d.rw = mk_rsrc(p.wo[br] + (size_t)(hg * 1024 + team * 128 + 16 * w) * (DM * 2) + nq * 320 - WOBIAS, (1024 - team * 128 - 16 * w) * (DM * 2) - nq * 320 + WOBIAS);
    d.m0w = lds + w * 5120;
#pragma unroll
    for (int P = 0; P < 5; ++P) {
      const int blk = 4 * P + (lane >> 4), kq = blk / 5, nb = blk % 5;
      d.voffW[P] = (4 * kq + q4) * (DM * 2) + nb * 64 + p4 * 16 + WOBIAS - (P & 3) * 1024;
    }
    astep<true, true, true, true, false, false, 8>(c, a, soff, slot, ns, bs, pf, s1, s0, pa, pb, [&](auto pc) {
      constexpr int P = decltype(pc)::value; wo_piece<0, P>(d); if constexpr (P == 3) wo_piece<0, 4>(d); });
    slot = ns; ns = nxt(slot);
    astep<true, true, false, true, false, false, 9>(c, a, soff, slot, ns, bs, pf, s0, s1, pb, pa, [&](auto pc) {
      constexpr int P = decltype(pc)::value; wo_piece<1, P>(d); if constexpr (P == 3) wo_piece<1, 4>(d); });
    slot = ns; ns = nxt(slot);
    astep<false, true, false, true, false, false, 10>(c, a, soff, slot, ns, bs, pf, s1, s0, pa, pb);
    slot = ns; ns = nxt(slot);
    astep<false, false, false, false, false, false, -1>(c, a, soff, slot, ns, bs, pf, s0, s1, pb, pa);
  }
  d.ro = mk_rsrc(p.ows + ((size_t)((br * NB + b) * 4 + w) * NH + 8 * hg + team) * 8192, 65536 - team * 8192);
  d.voffO = lane * 16;
  d.tbase = lds + h * 1280 + q4 * 64 + g1 * 32 + p4 * 8;
  bf16x8 o0[4], o1[4], o2[4], o3[4];
  auto put_o = [&]() {
    pack_o(a, o0, o1);
#pragma unroll
    for (int f = 0; f < 4; ++f) {
      __builtin_amdgcn_raw_buffer_store_b128(__builtin_bit_cast(u32x4, o0[f]), d.ro, d.voffO + f * 1024, pair * 16384, 16);
      __builtin_amdgcn_raw_buffer_store_b128(__builtin_bit_cast(u32x4, o1[f]), d.ro, d.voffO + 4096 + f * 1024, pair * 16384, 16);
    }
  };
  put_o();
  {
    const unsigned gw = lds + K2SLOT + 3 * 5120 + 4096;
    float big = a.l;
#pragma unroll
    for (int dt = 0; dt < 4; ++dt)
#pragma unroll
      for (int i = 0; i < 16; i += 2)
        big = __builtin_elementwise_maximum(big, __builtin_elementwise_maximum(fabsf(a.o[dt][i]), fabsf(a.o[dt][i + 1])));
    const bool bad = __any(!(big < 1.0e36f));
    if (lane == 0) *(LAS int*)(size_t)(gw + 4 * w) = bad;
    wait_bar<8>();
    const u32x4 fl = *(LAS u32x4*)(size_t)gw;
    if (__builtin_expect(__builtin_amdgcn_readfirstlane(fl[0] | fl[1] | fl[2] | fl[3]), 0)) { attn_safe(c, a); put_o(); wait_bar<0>(); }
  }

#pragma unroll
  for (int k = 0; k < 4; ++k) pf[k] = rdWo(d.tbase + 2 * K2SLOT, k);
  sfor<0, 5>([&](auto P) { wo_piece<2, decltype(P)::value>(d); });
  sfor<0, 3>([&](auto P) { wo_piece<3, decltype(P)::value>(d); });
  auto publish = [&] { if (w == 0 && lane == 0) __hip_atomic_store((gu64*)(flags + 64 + cl + pair), tag, __ATOMIC_RELAXED, __HIP_MEMORY_SCOPE_AGENT); };
  if (team == 0) {
    __builtin_amdgcn_s_barrier();
    __builtin_amdgcn_s_barrier();
    asm volatile("s_waitcnt vmcnt(0)\n\ts_barrier" ::: "memory");
    publish();
  }
  f32x16 acc[5] = {};
  auto nop = [] {};
  k2step<0, 0, false>(d, acc, pf, o0, o3, o1, publish);
  k2step<1, 63, false>(d, acc, pf, o1, o0, o2, nop);
  wait_flags<4>(flags + 64 + cl, tag, lds + 3 * K2SLOT + 4096, w, lane);
  oload4(d, 2, o2); oload4(d, 3, o3); oload4(d, 4, o0);
  wait_bar_o<8>(o2);
  k2step<2, 10, true>(d, acc, pf, o2, o1, o3, nop);
  k2step<3, 15, true>(d, acc, pf, o3, o2, o0, nop);
  k2step<4, 18, true>(d, acc, pf, o0, o3, o1, nop);
  k2step<5, 9, false>(d, acc, pf, o1, o0, o2, nop);
  k2step<6, 0, false>(d, acc, pf, o2, o1, o3, nop);
  k2step<7, -1, false>(d, acc, pf, o3, o2, o0, nop);

  const rsrc_t rp = mk_rsrc(p.ows + WS_PART, 512 * 40960);
  const int tl8 = ((br * NB + b) * 4 + nq) * 8;
  {
    const unsigned pw_ = (unsigned)(((tl8 + hg * 2 + team) * 4 + w) * 10240) + lane * 16;
#pragma unroll
    for (int f = 0; f < 10; ++f) __builtin_amdgcn_raw_buffer_store_b128(__builtin_bit_cast(u32x4, pack8(acc[f >> 1], f & 1, 1.0f)), rp, pw_ + f * 1024, 0, 16);
    asm volatile("s_waitcnt vmcnt(0)" ::: "memory");
    wait_bar<0>();
    if (w == 0 && lane == 0) __hip_atomic_store((gu64*)(flags + 576 + tl8 + hg * 2 + team), tag, __ATOMIC_RELAXED, __HIP_MEMORY_SCOPE_AGENT);
  }
  if (team) {
    wait_flags<8>(flags + 576 + tl8, tag, lds, w, lane);
    u32x4 v[3][8];
#pragma unroll
    for (int k = 0; k < 3; ++k)
#pragma unroll
      for (int g2 = 0; g2 < 8; ++g2) {
        const int f = w + 4 * k;
        v[k][g2] = __builtin_amdgcn_raw_buffer_load_b128(rp, lane * 16 + (f < 10 ? 0u : 0x40000000u), (unsigned)(((tl8 + g2) * 4 + hg) * 10240 + f * 1024), 16);
      }
#pragma unroll
    for (int k = 0; k < 3; ++k) {
      const int f = w + 4 * k;
      float sm[8] = {};
#pragma unroll
      for (int g2 = 0; g2 < 8; ++g2)
#pragma unroll
        for (int e = 0; e < 4; ++e) { sm[2 * e] += __uint_as_float(v[k][g2][e] << 16); sm[2 * e + 1] += __uint_as_float(v[k][g2][e] & 0xffff0000u); }
      if (f < 10) {
        const int q = b * 128 + hg * 32 + r, n = br * 640 + nq * 160 + 16 * f + 4 * h;
        char* o = p.out + ((size_t)q * 1280 + n) * 2;
        *(u32x2*)o = (u32x2){cvtpk(sm[0], sm[1]), cvtpk(sm[2], sm[3])};
        *(u32x2*)(o + 16) = (u32x2){cvtpk(sm[4], sm[5]), cvtpk(sm[6], sm[7])};
      }
    }
    if (bid == 0 && w == 1) {
      for (int it = 0; it < (1 << 15); ++it) {
        bool ok = true;
#pragma unroll
        for (int k = 0; k < 8; ++k) ok &= __hip_atomic_load((gu64*)(flags + 64 + k * 64 + lane), __ATOMIC_RELAXED, __HIP_MEMORY_SCOPE_AGENT) == tag;
        if (__all(ok)) break;
      }
      if (lane == 0) __hip_atomic_store((gu64*)flags, tag, __ATOMIC_RELAXED, __HIP_MEMORY_SCOPE_AGENT);
    }
  }
}

__global__ __launch_bounds__(512, 2) void fused_attn_kernel(Params p) {
  if ((blockIdx.x >> 3) & 1) body<1>(p); else body<0>(p);
}

extern "C" void kernel_launch(void* const* d_in, const int* in_sizes, int n_in, void* d_out, int out_size, void* d_ws, size_t ws_size,
                              hipStream_t stream) {
  Params p;
  for (int i = 0; i < 2; ++i) {
    p.x[i] = (const char*)d_in[5 * i + 0]; p.k[i] = (const char*)d_in[5 * i + 1]; p.v[i] = (const char*)d_in[5 * i + 2];
    p.wq[i] = (const char*)d_in[5 * i + 3]; p.wo[i] = (const char*)d_in[5 * i + 4];
  }
  p.ows = (char*)d_ws;
  p.out = (char*)d_out;
  (void)hipFuncSetAttribute((const void*)fused_attn_kernel, hipFuncAttributeMaxDynamicSharedMemorySize, 2 * RING);
  hipLaunchKernelGGL(fused_attn_kernel, dim3(256), dim3(512), 2 * RING, stream, p);
}
```
